```python
import torch

def _softmax(x, axis=-1):
    return torch.softmax(x, dim=axis)

def _gelu(x):
    return torch.nn.functional.gelu(x, approximate='tanh')

def _silu(x):
    return torch.nn.functional.silu(x)

def _sigmoid(x):
    return torch.sigmoid(x)

def _layernorm(x, axis=-1, eps=1e-05):
    m = x.mean(dim=axis, keepdim=True)
    v = x.var(dim=axis, keepdim=True, unbiased=False)
    return (x - m) / torch.sqrt(v + eps)

def _scatter_add(tgt, idx, src, axis=0):
    return tgt.clone().index_add_(axis, idx, src)

def _segment_sum(src, seg, n):
    out = torch.zeros((n,) + src.shape[1:], dtype=src.dtype)
    return out.index_add_(0, seg, src)

def _pad(x, pw):
    flat = tuple((v for pair in reversed(pw) for v in pair))
    return torch.nn.functional.pad(x, flat)
kernel_name = 'adaLN_cond__grouped_kv_mqa__glu_gated_reglu__prompt'

def setup_inputs():

    _w = lambda a: (lambda t: t.to(torch.bfloat16) if t.is_floating_point() else t)(torch.as_tensor(a))

    def _A_setup():
        B, S, D, H = (1, 2048, 1024, 32)
        Dh = D // H
        return {'x': _w((lambda *s: torch.randn(*s, dtype=torch.bfloat16) * 0.05)(B, S, D)), 'wq': _w((lambda *s: torch.randn(*s, dtype=torch.bfloat16) * 0.05)(D, H * Dh)), 'wk': _w((lambda *s: torch.randn(*s, dtype=torch.bfloat16) * 0.05)(D, Dh)), 'wv': _w((lambda *s: torch.randn(*s, dtype=torch.bfloat16) * 0.05)(D, Dh)), 'wo': _w((lambda *s: torch.randn(*s, dtype=torch.bfloat16) * 0.05)(H * Dh, D))}
    _A_in = _A_setup()

    def _B_setup():
        B, S, D, F = (1, 2048, 1024, 3584)
        return {'x': _w((lambda *s: torch.randn(*s, dtype=torch.bfloat16) * 0.05)(B, S, D)), 'W_gate': _w((lambda *s: torch.randn(*s, dtype=torch.bfloat16) * 0.8)(D, F) * (2.0 / D) ** 0.5), 'W_up': _w((lambda *s: torch.randn(*s, dtype=torch.bfloat16) * 0.8)(D, F) * (2.0 / D) ** 0.5), 'W_down': _w((lambda *s: torch.randn(*s, dtype=torch.bfloat16) * 0.8)(F, D) * (1.0 / F) ** 0.5)}
    _B_in = _B_setup()
    return {'A_x': _A_in['x'], 'A_wq': _A_in['wq'], 'A_wk': _A_in['wk'], 'A_wv': _A_in['wv'], 'A_wo': _A_in['wo'], 'B_x': _B_in['x'], 'B_W_gate': _B_in['W_gate'], 'B_W_up': _B_in['W_up'], 'B_W_down': _B_in['W_down'], 'c': _w((lambda *s: torch.randn(*s, dtype=torch.bfloat16) * 0.1)(1, 1024) * 8.0), 'w_mod': _w((lambda *s: torch.randn(*s, dtype=torch.bfloat16) * 0.1)(1024, 6 * 1024) * 1024 ** (-0.5) * 8.0)}

def reference(A_x, A_wq, A_wk, A_wv, A_wo, B_x, B_W_gate, B_W_up, B_W_down, c, w_mod):

    def _A_ref(x, wq, wk, wv, wo):
        B, S, D = x.shape
        H = 32
        Dh = D // H
        q = (lambda x, *d: x.permute(*d))((x @ wq).reshape(B, S, H, Dh), 0, 2, 1, 3)
        k = x @ wk
        v = x @ wv
        scores = torch.einsum('bhqd,bkd->bhqk', q, k) * Dh ** (-0.5)
        p = _softmax(scores, axis=-1)
        o = torch.einsum('bhqk,bkd->bhqd', p, v)
        return (lambda x, *d: x.permute(*d))(o, 0, 2, 1, 3).reshape(B, S, H * Dh) @ wo

    def _B_ref(x, W_gate, W_up, W_down):
        gate = torch.einsum('bsd,df->bsf', x, W_gate)
        up = torch.einsum('bsd,df->bsf', x, W_up)
        gate = gate * (lambda x, dt: x.to(dt))(gate > 0, gate.dtype)
        h = gate * up
        return torch.einsum('bsf,fd->bsd', h, W_down)
    x = A_x
    mod = (_silu(c) @ w_mod).reshape(c.shape[0], 6, 1, -1)
    sa, ca, ga, sb, cb, gb = (mod[:, 0].clone(), mod[:, 1].clone(), mod[:, 2].clone(), mod[:, 3].clone(), mod[:, 4].clone(), mod[:, 5].clone())
    h = _layernorm(x, axis=-1) * (1 + ca) + sa
    x = x + ga * _A_ref(h, A_wq, A_wk, A_wv, A_wo)
    h = _layernorm(x, axis=-1) * (1 + cb) + sb
    x = x + gb * _B_ref(h, B_W_gate, B_W_up, B_W_down)
    return x
```

```cpp
#include <hip/hip_runtime.h>
#include <hip/hip_bf16.h>
#include <stdint.h>

typedef unsigned short u16;
typedef __bf16 bf16x8 __attribute__((ext_vector_type(8)));
typedef __bf16 bf16x16 __attribute__((ext_vector_type(16)));
typedef short s16x4 __attribute__((ext_vector_type(4)));
typedef short s16x8 __attribute__((ext_vector_type(8)));
typedef float f32x2 __attribute__((ext_vector_type(2)));
typedef float f32x4 __attribute__((ext_vector_type(4)));
typedef float f32x8 __attribute__((ext_vector_type(8)));
typedef float f32x16 __attribute__((ext_vector_type(16)));
typedef unsigned u32x2 __attribute__((ext_vector_type(2)));
typedef unsigned u32x4 __attribute__((ext_vector_type(4)));
#define LAS __attribute__((address_space(3)))
typedef LAS char* lptr;

constexpr int SEQ = 2048, DM = 1024, FF = 3584, NMOD = 6144;
constexpr int MOD_KS = 4;

constexpr size_t WS_MODP = 0;
constexpr size_t WS_ST1 = WS_MODP + MOD_KS * NMOD * 4;
constexpr size_t WS_KST = WS_ST1 + SEQ * 8;
constexpr size_t WS_UV = WS_KST + 1024;
constexpr size_t WS_ST2 = WS_UV + 2 * 2 * 2 * FF * 4;
constexpr size_t WS_WKV = WS_ST2 + SEQ * 16 * 8;
constexpr size_t WS_KV = WS_WKV + 64 * 2 * 1024;
constexpr size_t WS_H = WS_KV + 2048 + 64 * 4096 + 2048;
constexpr size_t WS_O = WS_H + (size_t)SEQ * DM * 2;
constexpr size_t WS_X1 = WS_O + (size_t)SEQ * DM * 2;
constexpr size_t WS_Y = WS_X1 + (size_t)SEQ * DM * 2;
constexpr size_t WS_ACT = WS_Y + (size_t)SEQ * DM * 2;
constexpr size_t WS_END = WS_ACT + (size_t)SEQ * FF * 2;

__device__ __forceinline__ float bf_lo(unsigned u) { return __uint_as_float(u << 16); }
__device__ __forceinline__ float bf_hi(unsigned u) { return __uint_as_float(u & 0xffff0000u); }
__device__ __forceinline__ unsigned pack2(float lo, float hi) {
  typedef __bf16 bf16x2 __attribute__((ext_vector_type(2)));
  f32x2 v = {lo, hi};
  bf16x2 r = __builtin_convertvector(v, bf16x2);
  return __builtin_bit_cast(unsigned, r);
}
__device__ __forceinline__ float rbf(float x) { return (float)(__bf16)x; }
__device__ __forceinline__ u32x4 make_srd(const void* p, unsigned bytes) {
  uint64_t a = (uint64_t)p;
  u32x4 r;
  r[0] = (unsigned)a; r[1] = (unsigned)(a >> 32); r[2] = bytes; r[3] = 0x00020000u;
  return r;
}
__device__ __forceinline__ void dma16(unsigned ldsaddr, unsigned voff, u32x4 srd, unsigned soff) {
  asm volatile("s_mov_b32 m0, %0\n\ts_nop 0\n\tbuffer_load_dwordx4 %1, %2, %3 offen lds"
               :: "s"(ldsaddr), "v"(voff), "s"(srd), "s"(soff) : "memory");
}
#define WAIT_VM(n) asm volatile("s_waitcnt vmcnt(" #n ")" ::: "memory")
#define WAIT_BAR(n) asm volatile("s_waitcnt vmcnt(" #n ")\n\ts_barrier" ::: "memory")
#define WAIT_ALL_BAR() asm volatile("s_waitcnt vmcnt(0) lgkmcnt(0)\n\ts_barrier" ::: "memory")
#define LGKM0_BAR() asm volatile("s_waitcnt lgkmcnt(0)\n\ts_barrier" ::: "memory")
__device__ __forceinline__ bf16x8 lds_frag(lptr p) { return *(LAS bf16x8*)p; }
__device__ __forceinline__ bf16x8 lds_tr2(lptr p0, lptr p1) {
  s16x4 a = __builtin_amdgcn_ds_read_tr16_b64_v4i16((LAS s16x4*)p0);
  s16x4 b = __builtin_amdgcn_ds_read_tr16_b64_v4i16((LAS s16x4*)p1);
  s16x8 r = __builtin_shufflevector(a, b, 0, 1, 2, 3, 4, 5, 6, 7);
  return __builtin_bit_cast(bf16x8, r);
}
#define MFMA16(a, b, c) __builtin_amdgcn_mfma_f32_16x16x32_bf16(a, b, c, 0, 0, 0)
#define MFMA32(a, b, c) __builtin_amdgcn_mfma_f32_32x32x16_bf16(a, b, c, 0, 0, 0)
__device__ __forceinline__ float swap32_sum(float v) {
  auto r = __builtin_amdgcn_permlane32_swap(__float_as_uint(v), __float_as_uint(v), false, false);
  return __uint_as_float(r[0]) + __uint_as_float(r[1]);
}
__device__ __forceinline__ float swap32_max(float v) {
  auto r = __builtin_amdgcn_permlane32_swap(__float_as_uint(v), __float_as_uint(v), false, false);
  return fmaxf(__uint_as_float(r[0]), __uint_as_float(r[1]));
}
__device__ __forceinline__ f32x4 mod4(const float* modp, int n) {
  f32x4 s = *(const f32x4*)(modp + n);
#pragma unroll
  for (int k = 1; k < MOD_KS; ++k) s += *(const f32x4*)(modp + k * NMOD + n);
  s[0] = rbf(s[0]); s[1] = rbf(s[1]); s[2] = rbf(s[2]); s[3] = rbf(s[3]);
  return s;
}

constexpr int K1_MOD_BLOCKS = (NMOD / 64) * MOD_KS;
constexpr int K1_ST_BLOCKS = SEQ / 8;
constexpr int K1_WKV_BLOCKS = 32;
__global__ __launch_bounds__(256) void k1_pre(const u16* __restrict__ c, const u16* __restrict__ wmod,
                                              const u16* __restrict__ x, const u16* __restrict__ wk,
                                              const u16* __restrict__ wv, char* __restrict__ ws) {
  __shared__ float red[4][64];
  const int tid = threadIdx.x, lane = tid & 63, w = tid >> 6;
  int b = blockIdx.x;
  if (b < K1_MOD_BLOCKS) {
    const int cg = b % (NMOD / 64), ks = b / (NMOD / 64);
    constexpr int ROWS = DM / MOD_KS;
    const int r0 = ks * ROWS + (tid >> 3);
    u32x4 wv4[ROWS / 32];
    float sc[ROWS / 32];
#pragma unroll
    for (int i = 0; i < ROWS / 32; ++i)
      wv4[i] = *(const u32x4*)(wmod + (size_t)(r0 + 32 * i) * NMOD + cg * 64 + (tid & 7) * 8);
#pragma unroll
    for (int i = 0; i < ROWS / 32; ++i) {
      float cv = bf_lo(c[r0 + 32 * i]);
      sc[i] = rbf(cv / (1.0f + __expf(-cv)));
    }
    float acc[8] = {0, 0, 0, 0, 0, 0, 0, 0};
#pragma unroll
    for (int i = 0; i < ROWS / 32; ++i)
#pragma unroll
      for (int j = 0; j < 4; ++j) {
        acc[2 * j] += sc[i] * bf_lo(wv4[i][j]);
        acc[2 * j + 1] += sc[i] * bf_hi(wv4[i][j]);
      }
#pragma unroll
    for (int j = 0; j < 8; ++j) {
      acc[j] += __shfl_xor(acc[j], 8);
      acc[j] += __shfl_xor(acc[j], 16);
      acc[j] += __shfl_xor(acc[j], 32);
    }
    if (lane < 8) {
#pragma unroll
      for (int j = 0; j < 8; ++j) red[w][lane * 8 + j] = acc[j];
    }
    __syncthreads();
    if (tid < 64) {
      float* modp = (float*)(ws + WS_MODP);
      modp[ks * NMOD + cg * 64 + tid] = (red[0][tid] + red[1][tid]) + (red[2][tid] + red[3][tid]);
    }
    return;
  }
  b -= K1_MOD_BLOCKS;
  if (b < K1_ST_BLOCKS) {
    u32x4 v[2][2];
#pragma unroll
    for (int r = 0; r < 2; ++r)
#pragma unroll
      for (int i = 0; i < 2; ++i)
        v[r][i] = *(const u32x4*)(x + (size_t)(b * 8 + w * 2 + r) * DM + i * 512 + lane * 8);
#pragma unroll
    for (int r = 0; r < 2; ++r) {
      float f[16];
#pragma unroll
      for (int i = 0; i < 2; ++i)
#pragma unroll
        for (int j = 0; j < 4; ++j) { f[i * 8 + 2 * j] = bf_lo(v[r][i][j]); f[i * 8 + 2 * j + 1] = bf_hi(v[r][i][j]); }
      float s = 0;
#pragma unroll
      for (int j = 0; j < 16; ++j) s += f[j];
#pragma unroll
      for (int o = 1; o < 64; o <<= 1) s += __shfl_xor(s, o);
      const float m = s * (1.0f / DM);
      float q = 0;
#pragma unroll
      for (int j = 0; j < 16; ++j) q += (f[j] - m) * (f[j] - m);
#pragma unroll
      for (int o = 1; o < 64; o <<= 1) q += __shfl_xor(q, o);
      if (lane == 0) {
        f32x2 st = {m, 1.0f / sqrtf(q * (1.0f / DM) + 1e-5f)};
        *(f32x2*)(ws + WS_ST1 + (size_t)(b * 8 + w * 2 + r) * 8) = st;
      }
    }
    return;
  }
  b -= K1_ST_BLOCKS;
  {
    const int idx = b * 256 + tid;
    const int l = idx & 63, mat = (idx >> 6) & 1, s16 = idx >> 7;
    const u16* W = mat ? wv : wk;
    const u16* p = W + (size_t)(16 * s16 + 8 * (l >> 5)) * 32 + (l & 31);
    u32x4 o;
#pragma unroll
    for (int j = 0; j < 4; ++j) o[j] = (unsigned)p[(2 * j) * 32] | ((unsigned)p[(2 * j + 1) * 32] << 16);
    *(u32x4*)(ws + WS_WKV + (size_t)idx * 16) = o;
  }
}

__global__ __launch_bounds__(512) void k2_ln_kv(const u16* __restrict__ x, char* __restrict__ ws) {
  __shared__ __attribute__((aligned(16))) float smem[2048 + 8 * 2 * 16 * 64];
  float* smod = smem;
  float* sred = smem + 2048;
  const int tid = threadIdx.x, lane = tid & 63, w = tid >> 6, T = blockIdx.x;
  const int tok = T * 32 + (lane & 31), h = lane >> 5;
  u32x4 xv[8];
  bf16x8 wf[8][2];
#pragma unroll
  for (int i = 0; i < 8; ++i) xv[i] = *(const u32x4*)(x + (size_t)tok * DM + (8 * w + i) * 16 + 8 * h);
  const f32x2 st = *(const f32x2*)(ws + WS_ST1 + (size_t)tok * 8);
  {
    f32x4 m = mod4((const float*)(ws + WS_MODP), tid * 4);
    if (tid >= 256) { m[0] = rbf(1.0f + m[0]); m[1] = rbf(1.0f + m[1]); m[2] = rbf(1.0f + m[2]); m[3] = rbf(1.0f + m[3]); }
    *(f32x4*)(smod + tid * 4) = m;
  }
#pragma unroll
  for (int i = 0; i < 8; ++i)
#pragma unroll
    for (int m = 0; m < 2; ++m)
      wf[i][m] = *(const bf16x8*)(ws + WS_WKV + ((size_t)((8 * w + i) * 2 + m) * 64 + lane) * 16);
  __syncthreads();
  f32x16 ak = {}, av = {};
  const float rs = st[1], nm = -st[0] * st[1];
#pragma unroll
  for (int i = 0; i < 8; ++i) {
    const int k0 = (8 * w + i) * 16 + 8 * h;
    const f32x4 sa0 = *(const f32x4*)(smod + k0), sa1 = *(const f32x4*)(smod + k0 + 4);
    const f32x4 ca0 = *(const f32x4*)(smod + 1024 + k0), ca1 = *(const f32x4*)(smod + 1024 + k0 + 4);
    u32x4 hv;
    hv[0] = pack2((bf_lo(xv[i][0]) * rs + nm) * ca0[0] + sa0[0], (bf_hi(xv[i][0]) * rs + nm) * ca0[1] + sa0[1]);
    hv[1] = pack2((bf_lo(xv[i][1]) * rs + nm) * ca0[2] + sa0[2], (bf_hi(xv[i][1]) * rs + nm) * ca0[3] + sa0[3]);
    hv[2] = pack2((bf_lo(xv[i][2]) * rs + nm) * ca1[0] + sa1[0], (bf_hi(xv[i][2]) * rs + nm) * ca1[1] + sa1[1]);
    hv[3] = pack2((bf_lo(xv[i][3]) * rs + nm) * ca1[2] + sa1[2], (bf_hi(xv[i][3]) * rs + nm) * ca1[3] + sa1[3]);
    *(u32x4*)(ws + WS_H + ((size_t)(T * 64 + 8 * w + i) * 64 + lane) * 16) = hv;
    const bf16x8 hf = __builtin_bit_cast(bf16x8, hv);
    ak = MFMA32(wf[i][0], hf, ak);
    av = MFMA32(hf, wf[i][1], av);
  }
#pragma unroll
  for (int q = 0; q < 4; ++q) {
    *(f32x4*)(sred + (((w * 2 + 0) * 4 + q) * 64 + lane) * 4) = (f32x4){ak[4 * q], ak[4 * q + 1], ak[4 * q + 2], ak[4 * q + 3]};
    *(f32x4*)(sred + (((w * 2 + 1) * 4 + q) * 64 + lane) * 4) = (f32x4){av[4 * q], av[4 * q + 1], av[4 * q + 2], av[4 * q + 3]};
  }
  __syncthreads();
  if (w < 2) {
    f32x4 r[4];
#pragma unroll
    for (int q = 0; q < 4; ++q) {
      const int sq = (w == 0) ? q : (2 * (q >> 1) + h);
      const int sl = (w == 0) ? lane : ((lane & 31) | ((q & 1) << 5));
      f32x4 s = {0, 0, 0, 0};
#pragma unroll
      for (int ww = 0; ww < 8; ++ww) s += *(const f32x4*)(sred + (((ww * 2 + w) * 4 + sq) * 64 + sl) * 4);
      r[q] = s;
    }
    u32x4 p0 = {pack2(r[0][0], r[0][1]), pack2(r[0][2], r[0][3]), pack2(r[1][0], r[1][1]), pack2(r[1][2], r[1][3])};
    u32x4 p1 = {pack2(r[2][0], r[2][1]), pack2(r[2][2], r[2][3]), pack2(r[3][0], r[3][1]), pack2(r[3][2], r[3][3])};
    char* kv = ws + WS_KV + 2048;
    char* dst = (w == 0) ? (T == 0 ? kv - 2048 : kv + (size_t)(T - 1) * 4096) : (kv + (size_t)T * 4096 + 2048);
    *(u32x4*)(dst + lane * 16) = p0;
    *(u32x4*)(dst + 1024 + lane * 16) = p1;
    float red = 0;
#pragma unroll
    for (int j = 0; j < 4; ++j) {
      const float a = bf_lo(p0[j]), bq = bf_hi(p0[j]), cq = bf_lo(p1[j]), d = bf_hi(p1[j]);
      if (w == 0) red += a * a + bq * bq + cq * cq + d * d;
      else red = fmaxf(fmaxf(red, fmaxf(fabsf(a), fabsf(bq))), fmaxf(fabsf(cq), fabsf(d)));
      if (w == 1 && !(a * 0.f + bq * 0.f + cq * 0.f + d * 0.f == 0.f)) red = __builtin_inff();
    }
    if (w == 0) { red = swap32_sum(red); if (!(red == red)) red = __builtin_inff(); }
#pragma unroll
    for (int o = 1; o < 64; o <<= 1) red = fmaxf(red, __shfl_xor(red, o));
    if (lane == 0) ((float*)(ws + WS_KST))[T * 2 + w] = red;
    if (w == 0 && T == 63) {
      *(u32x4*)(kv + (size_t)63 * 4096 + lane * 16) = p0;
      *(u32x4*)(kv + (size_t)63 * 4096 + 1024 + lane * 16) = p1;
    }
  }
}

__global__ __launch_bounds__(256) void k2b_uv(const u16* __restrict__ wg, const u16* __restrict__ wu, char* __restrict__ ws) {
  __shared__ float red[2][4][64];
  __shared__ float smod[2][512];
  const int tid = threadIdx.x, lane = tid & 63, w = tid >> 6;
  const int cg = blockIdx.x % 112, ks = blockIdx.x / 112;
  const u16* W = (cg < 56) ? wg + cg * 64 : wu + (cg - 56) * 64;
  if (tid < 128) {
    f32x4 m = mod4((const float*)(ws + WS_MODP), 3 * DM + ks * 512 + tid * 4);
    *(f32x4*)(&smod[1][tid * 4]) = m;
  } else {
    f32x4 m = mod4((const float*)(ws + WS_MODP), 4 * DM + ks * 512 + (tid - 128) * 4);
    m[0] = rbf(1.0f + m[0]); m[1] = rbf(1.0f + m[1]); m[2] = rbf(1.0f + m[2]); m[3] = rbf(1.0f + m[3]);
    *(f32x4*)(&smod[0][(tid - 128) * 4]) = m;
  }
  u32x4 wv4[16];
#pragma unroll
  for (int i = 0; i < 16; ++i) wv4[i] = *(const u32x4*)(W + (size_t)(ks * 512 + (tid >> 3) + 32 * i) * FF + (tid & 7) * 8);
  __syncthreads();
  float au[8] = {0, 0, 0, 0, 0, 0, 0, 0}, avv[8] = {0, 0, 0, 0, 0, 0, 0, 0};
#pragma unroll
  for (int i = 0; i < 16; ++i) {
    const float a = smod[0][(tid >> 3) + 32 * i], s = smod[1][(tid >> 3) + 32 * i];
#pragma unroll
    for (int j = 0; j < 4; ++j) {
      const float lo = bf_lo(wv4[i][j]), hi = bf_hi(wv4[i][j]);
      au[2 * j] += a * lo; au[2 * j + 1] += a * hi;
      avv[2 * j] += s * lo; avv[2 * j + 1] += s * hi;
    }
  }
#pragma unroll
  for (int j = 0; j < 8; ++j) {
    au[j] += __shfl_xor(au[j], 8); au[j] += __shfl_xor(au[j], 16); au[j] += __shfl_xor(au[j], 32);
    avv[j] += __shfl_xor(avv[j], 8); avv[j] += __shfl_xor(avv[j], 16); avv[j] += __shfl_xor(avv[j], 32);
  }
  if (lane < 8) {
#pragma unroll
    for (int j = 0; j < 8; ++j) { red[0][w][lane * 8 + j] = au[j]; red[1][w][lane * 8 + j] = avv[j]; }
  }
  __syncthreads();
  if (tid < 128) {
    const int m = tid >> 6, cidx = tid & 63;
    float* uv = (float*)(ws + WS_UV);
    uv[(ks * 2 + m) * (2 * FF) + cg * 64 + cidx] = (red[m][0][cidx] + red[m][1][cidx]) + (red[m][2][cidx] + red[m][3][cidx]);
  }
}

constexpr int K3_LDS = 160 * 1024;
constexpr float QSCALE = 0.17677669529663687f * 1.4426950408889634f;
__device__ __forceinline__ bf16x16 as_b16(const f32x16& p, int half) {
  f32x8 t = half ? __builtin_shufflevector(p, p, 8, 9, 10, 11, 12, 13, 14, 15) : __builtin_shufflevector(p, p, 0, 1, 2, 3, 4, 5, 6, 7);
  return __builtin_bit_cast(bf16x16, t);
}
#define SPIDX 0xDDDDDDDD
#define SMF32(a, b, c) __builtin_amdgcn_smfmac_f32_32x32x32_bf16(a, b, c, (int)SPIDX, 0, 0)
#define SMF16(a, b, c) __builtin_amdgcn_smfmac_f32_16x16x64_bf16(a, b, c, (int)SPIDX, 0, 0)

__global__ __launch_bounds__(512, 2) void k3_attn(const u16* __restrict__ wq, char* __restrict__ ws) {
  extern __shared__ __attribute__((aligned(16))) char smem_raw[];
  lptr lds = (lptr)smem_raw;
  const unsigned lds0 = (unsigned)(uintptr_t)lds;
  const int tid = threadIdx.x, lane = tid & 63;
  const int w = __builtin_amdgcn_readfirstlane(tid >> 6);
  const int xcd = blockIdx.x & 7, loc = blockIdx.x >> 3;
  const int tg = (xcd & 3) * 4 + (loc & 3);
  const int hp = (xcd >> 2) * 8 + (loc >> 2);
  const int tt = w & 3, hh = w >> 2;
  const u32x4 srdH = make_srd(ws + WS_H, SEQ * DM * 2);
  const u32x4 srdW = make_srd(wq, DM * DM * 2);
  const u32x4 srdKV = make_srd(ws + WS_KV + 2048, 64 * 4096);
  const unsigned vlin = lane * 16;
  dma16(lds0 + 9 * 16384 + w * 2048, vlin, srdKV, w * 2048);
  dma16(lds0 + 9 * 16384 + w * 2048 + 1024, vlin, srdKV, w * 2048 + 1024);
  unsigned vW;
  {
    const int rho = lane >> 3, cp = lane & 7;
    const int kk = 8 * ((rho >> 2) & 1) + (rho & 3);
    const int ch = cp ^ (((rho >> 1) & 1) << 2);
    vW = kk * (DM * 2) + ch * 16;
  }
  const unsigned sT0 = (unsigned)((tg * 4 + (w >> 1)) * 64 + (w & 1) * 4) * 1024;
  const unsigned sW0 = (unsigned)(w * 16) * (DM * 2) + hp * 128;
  const unsigned dT = (w >> 1) * 8192 + (w & 1) * 4096, dW = 32768 + w * 2048;
#define K3_STAGE(st, slot) do { const unsigned b_ = lds0 + (slot) * 49152; \
    dma16(b_ + dT, vlin, srdH, sT0 + (st) * 8192); dma16(b_ + dT + 1024, vlin, srdH, sT0 + (st) * 8192 + 1024); \
    dma16(b_ + dT + 2048, vlin, srdH, sT0 + (st) * 8192 + 2048); dma16(b_ + dT + 3072, vlin, srdH, sT0 + (st) * 8192 + 3072); \
    dma16(b_ + dW, vW, srdW, sW0 + (st) * (128 * DM * 2)); dma16(b_ + dW + 1024, vW, srdW, sW0 + (st) * (128 * DM * 2) + 4 * DM * 2); } while (0)
  K3_STAGE(0, 0);
  K3_STAGE(1, 1);
  const f32x2 kst = *(const f32x2*)(ws + WS_KST + lane * 8);
  const bf16x8 k00 = *(const bf16x8*)(ws + WS_KV + lane * 16), k01 = *(const bf16x8*)(ws + WS_KV + 1024 + lane * 16);
  float kmax2 = kst[0], vmax = kst[1];
#pragma unroll
  for (int o = 1; o < 64; o <<= 1) { kmax2 = fmaxf(kmax2, __shfl_xor(kmax2, o)); vmax = fmaxf(vmax, __shfl_xor(vmax, o)); }
  f32x16 qa = {};
  {
    const int q = (lane & 15) >> 2, p = lane & 3, g1 = (lane >> 4) & 1, h = lane >> 5;
    const unsigned aoff = 32768 + (4 * h + q) * 128 + ((hh ^ ((q >> 1) & 1)) << 6) + g1 * 32 + p * 8;
    const unsigned boff = tt * 8192 + lane * 16;
    int slot = 0;
    for (int st = 0; st < 8; ++st) {
      if (st < 7) { asm volatile("s_waitcnt vmcnt(6) lgkmcnt(0)\n\ts_barrier" ::: "memory"); } else { WAIT_BAR(0); }
      if (st < 6) { const int sl2 = (slot + 2) % 3; K3_STAGE(st + 2, sl2); }
      lptr base = lds + slot * 49152;
#pragma unroll
      for (int s = 0; s < 8; ++s) {
        const bf16x8 a = lds_tr2(base + aoff + s * 2048, base + aoff + s * 2048 + 1024);
        const bf16x8 b = lds_frag(base + boff + s * 1024);
        qa = MFMA32(a, b, qa);
      }
      slot = (slot == 2) ? 0 : slot + 1;
    }
  }
  LGKM0_BAR();
#pragma unroll
  for (int c = 1; c <= 9; ++c) {
    dma16(lds0 + (c - 1) * 16384 + w * 2048, vlin, srdKV, c * 16384 + w * 2048);
    dma16(lds0 + (c - 1) * 16384 + w * 2048 + 1024, vlin, srdKV, c * 16384 + w * 2048 + 1024);
  }
  bf16x8 q0, q1;
  float qn = 0;
  {
    u32x4 a, b;
#pragma unroll
    for (int j = 0; j < 4; ++j) {
      a[j] = pack2(qa[2 * j] * QSCALE, qa[2 * j + 1] * QSCALE);
      b[j] = pack2(qa[8 + 2 * j] * QSCALE, qa[8 + 2 * j + 1] * QSCALE);
      qn += bf_lo(a[j]) * bf_lo(a[j]) + bf_hi(a[j]) * bf_hi(a[j]) + bf_lo(b[j]) * bf_lo(b[j]) + bf_hi(b[j]) * bf_hi(b[j]);
    }
    q0 = __builtin_bit_cast(bf16x8, a); q1 = __builtin_bit_cast(bf16x8, b);
    qn = swap32_sum(qn);
  }
  f32x16 sA = MFMA32(k00, q0, (f32x16){});
  sA = MFMA32(k01, q1, sA);
  float m = sA[0];
#pragma unroll
  for (int r = 1; r < 16; ++r) m = fmaxf(m, sA[r]);
  m = swap32_max(m);
  const bool safe = (sqrtf(qn * kmax2) * 1.01f - m <= 90.0f) && (vmax <= 16777216.0f);
  const bool fast = __all(safe);
  bf16x8 ones;
  {
    const int row = lane & 15;
    const unsigned lo = ((row & 7) == 0) ? 0x3f803f80u : 0u, hi = ((row & 7) == 4) ? 0x3f803f80u : 0u;
    u32x4 o = {lo, lo, hi, hi};
    ones = __builtin_bit_cast(bf16x8, o);
  }
  f32x16 oacc = {};
  f32x4 lacc = {0, 0, 0, 0};
  f32x16 negm;
#pragma unroll
  for (int r = 0; r < 16; ++r) { negm[r] = -m; sA[r] -= m; }
  f32x16 sB;
#define UNIT_FAST(SC, SN, up) do { \
    const bf16x8 kf0 = lds_frag((up)), kf1 = lds_frag((up) + 1024), vf0 = lds_frag((up) + 2048), vf1 = lds_frag((up) + 3072); \
    SN = MFMA32(kf0, q0, negm); SN = MFMA32(kf1, q1, SN); \
    _Pragma("unroll") for (int r = 0; r < 16; ++r) SC[r] = __builtin_amdgcn_exp2f(SC[r]); \
    oacc = SMF32(vf0, as_b16(SC, 0), oacc); lacc = SMF16(ones, as_b16(SC, 0), lacc); \
    oacc = SMF32(vf1, as_b16(SC, 1), oacc); lacc = SMF16(ones, as_b16(SC, 1), lacc); } while (0)
#define UNIT_SLOW(SC, SN, up) do { \
    const bf16x8 kf0 = lds_frag((up)), kf1 = lds_frag((up) + 1024), vf0 = lds_frag((up) + 2048), vf1 = lds_frag((up) + 3072); \
    float t_ = SC[0]; \
    _Pragma("unroll") for (int r = 1; r < 16; ++r) t_ = fmaxf(t_, SC[r]); \
    t_ = fmaxf(swap32_max(t_), 0.0f); \
    const float al_ = __builtin_amdgcn_exp2f(-t_); \
    _Pragma("unroll") for (int r = 0; r < 16; ++r) { SC[r] = __builtin_amdgcn_exp2f(SC[r] - t_); oacc[r] *= al_; negm[r] -= t_; } \
    lacc[0] *= al_; \
    SN = MFMA32(kf0, q0, negm); SN = MFMA32(kf1, q1, SN); \
    oacc = SMF32(vf0, as_b16(SC, 0), oacc); lacc = SMF16(ones, as_b16(SC, 0), lacc); \
    oacc = SMF32(vf1, as_b16(SC, 1), oacc); lacc = SMF16(ones, as_b16(SC, 1), lacc); } while (0)
  int cslot = 9;
  for (int c = 0; c < 16; ++c) {
    if (c >= 1 && c <= 7) {
      if (c < 7) {
        asm volatile("s_waitcnt vmcnt(16) lgkmcnt(0)\n\ts_barrier" ::: "memory");
        const int fs = (cslot == 0) ? 9 : cslot - 1;
        dma16(lds0 + fs * 16384 + w * 2048, vlin, srdKV, (c + 9) * 16384 + w * 2048);
        dma16(lds0 + fs * 16384 + w * 2048 + 1024, vlin, srdKV, (c + 9) * 16384 + w * 2048 + 1024);
      } else {
        WAIT_BAR(0);
      }
    }
    lptr cb = lds + cslot * 16384 + lane * 16;
    if (fast) {
      UNIT_FAST(sA, sB, cb); UNIT_FAST(sB, sA, cb + 4096); UNIT_FAST(sA, sB, cb + 8192); UNIT_FAST(sB, sA, cb + 12288);
    } else {
      UNIT_SLOW(sA, sB, cb); UNIT_SLOW(sB, sA, cb + 4096); UNIT_SLOW(sA, sB, cb + 8192); UNIT_SLOW(sB, sA, cb + 12288);
    }
    cslot = (cslot == 9) ? 0 : cslot + 1;
  }
  {
    const float inv = 1.0f / lacc[0];
    const int tok = tg * 128 + tt * 32 + (lane & 31), h = lane >> 5, head = hp * 2 + hh;
    char* dst = ws + WS_O + ((size_t)((tok >> 4) * 32 + head) * 64 + h * 16 + (tok & 15)) * 16;
    u32x4 a, b;
#pragma unroll
    for (int j = 0; j < 4; ++j) {
      a[j] = pack2(oacc[2 * j] * inv, oacc[2 * j + 1] * inv);
      b[j] = pack2(oacc[8 + 2 * j] * inv, oacc[8 + 2 * j + 1] * inv);
    }
    *(u32x4*)dst = a;
    *(u32x4*)(dst + 512) = b;
  }
}

constexpr int KG_LDS = 3 * 49152;
template <int KDIM, bool FINAL>
__global__ __launch_bounds__(512, 2) void kg_gemm(const char* __restrict__ tokens, const u16* __restrict__ W,
                                                  const u16* __restrict__ resid, u16* __restrict__ outp,
                                                  char* __restrict__ ws) {
  extern __shared__ __attribute__((aligned(16))) char smem_raw[];
  lptr lds = (lptr)smem_raw;
  const unsigned lds0 = (unsigned)(uintptr_t)lds;
  constexpr int NS32 = KDIM / 32, NST = KDIM / 128;
  const int tid = threadIdx.x, lane = tid & 63;
  const int w = __builtin_amdgcn_readfirstlane(tid >> 6);
  const int xcd = blockIdx.x & 7, loc = blockIdx.x >> 3;
  const int tm = (xcd & 3) * 4 + (loc & 3);
  const int tn = (xcd >> 2) * 8 + (loc >> 2);
  const int kgp = w >> 1, mh = w & 1;
  const u32x4 srdT = make_srd(tokens, (unsigned)SEQ * KDIM * 2);
  const u32x4 srdW = make_srd(W, (unsigned)KDIM * DM * 2);
  const unsigned vlin = lane * 16;
  unsigned vW;
  {
    const int rl = lane >> 3, cp = lane & 7;
    const int ch = cp ^ (((rl >> 1) & 3) << 1);
    vW = rl * (DM * 2) + ch * 16;
  }
  const unsigned sT0 = (unsigned)(tm * 8 + w) * NS32 * 1024;
  const unsigned sW0 = (unsigned)((w >> 1) * 32 + (w & 1) * 8) * (DM * 2) + tn * 128;
  const unsigned dT = w * 4096, dW = 32768 + w * 2048;
#define KG_STAGE(st, slot) do { const unsigned b_ = lds0 + (slot) * 49152; \
    dma16(b_ + dT, vlin, srdT, sT0 + (st) * 4096); dma16(b_ + dT + 1024, vlin, srdT, sT0 + (st) * 4096 + 1024); \
    dma16(b_ + dT + 2048, vlin, srdT, sT0 + (st) * 4096 + 2048); dma16(b_ + dT + 3072, vlin, srdT, sT0 + (st) * 4096 + 3072); \
    dma16(b_ + dW, vW, srdW, sW0 + (st) * (128 * DM * 2)); dma16(b_ + dW + 1024, vW, srdW, sW0 + (st) * (128 * DM * 2) + 16 * DM * 2); } while (0)
  KG_STAGE(0, 0);
  KG_STAGE(1, 1);
  f32x4 acc[4][4];
#pragma unroll
  for (int a = 0; a < 4; ++a)
#pragma unroll
    for (int b = 0; b < 4; ++b) acc[a][b] = (f32x4){0, 0, 0, 0};
  {
    const int g = lane >> 4, q = (lane & 15) >> 2, p = lane & 3;
    const int xs = (2 * g + (q >> 1)) & 3;
    unsigned aoff[4];
#pragma unroll
    for (int a = 0; a < 4; ++a) aoff[a] = 32768 + kgp * 4096 + (4 * g + q) * 128 + ((a ^ xs) << 5) + p * 8;
    const unsigned boff = mh * 16384 + kgp * 1024 + lane * 16;
    int slot = 0;
    for (int st = 0; st < NST; ++st) {
      if (st < NST - 1) { asm volatile("s_waitcnt vmcnt(6) lgkmcnt(0)\n\ts_barrier" ::: "memory"); } else { WAIT_BAR(0); }
      if (st < NST - 2) { const int sl2 = (slot == 0) ? 2 : slot - 1; KG_STAGE(st + 2, sl2); }
      lptr base = lds + slot * 49152;
      bf16x8 af[4], bfr[4];
#pragma unroll
      for (int a = 0; a < 4; ++a) af[a] = lds_tr2(base + aoff[a], base + aoff[a] + 2048);
#pragma unroll
      for (int b = 0; b < 4; ++b) bfr[b] = lds_frag(base + boff + b * 4096);
#pragma unroll
      for (int a = 0; a < 4; ++a)
#pragma unroll
        for (int b = 0; b < 4; ++b) acc[a][b] = MFMA16(af[a], bfr[b], acc[a][b]);
      slot = (slot == 2) ? 0 : slot + 1;
    }
  }
  const int etok = tid >> 3, ech = tid & 7;
  const int ncol = tn * 64 + ech * 8;
  u32x4 rv[2];
#pragma unroll
  for (int i = 0; i < 2; ++i) rv[i] = *(const u32x4*)(resid + (size_t)(tm * 128 + i * 64 + etok) * DM + ncol);
  const float* modp = (const float*)(ws + WS_MODP);
  const f32x4 g0 = mod4(modp, (FINAL ? 5 : 2) * DM + ncol), g1 = mod4(modp, (FINAL ? 5 : 2) * DM + ncol + 4);
  f32x4 c0, c1;
  if (!FINAL) {
    c0 = mod4(modp, 4 * DM + ncol); c1 = mod4(modp, 4 * DM + ncol + 4);
#pragma unroll
    for (int j = 0; j < 4; ++j) { c0[j] = rbf(1.0f + c0[j]); c1[j] = rbf(1.0f + c1[j]); }
  }
  LGKM0_BAR();
  {
    const int g = lane >> 4, cidx = lane & 15;
#pragma unroll
    for (int a = 0; a < 4; ++a)
#pragma unroll
      for (int b = 0; b < 4; ++b)
        *(LAS f32x4*)(lds + (kgp * 128 + mh * 64 + b * 16 + cidx) * 272 + (a * 16 + 4 * g) * 4) = acc[a][b];
  }
  LGKM0_BAR();
#pragma unroll
  for (int i = 0; i < 2; ++i) {
    const int tl = i * 64 + etok, tok = tm * 128 + tl;
    f32x4 s0 = {0, 0, 0, 0}, s1 = {0, 0, 0, 0};
#pragma unroll
    for (int k = 0; k < 4; ++k) {
      s0 += *(LAS f32x4*)(lds + (k * 128 + tl) * 272 + ech * 32);
      s1 += *(LAS f32x4*)(lds + (k * 128 + tl) * 272 + ech * 32 + 16);
    }
    u32x4 o;
    o[0] = pack2(bf_lo(rv[i][0]) + g0[0] * s0[0], bf_hi(rv[i][0]) + g0[1] * s0[1]);
    o[1] = pack2(bf_lo(rv[i][1]) + g0[2] * s0[2], bf_hi(rv[i][1]) + g0[3] * s0[3]);
    o[2] = pack2(bf_lo(rv[i][2]) + g1[0] * s1[0], bf_hi(rv[i][2]) + g1[1] * s1[1]);
    o[3] = pack2(bf_lo(rv[i][3]) + g1[2] * s1[2], bf_hi(rv[i][3]) + g1[3] * s1[3]);
    *(u32x4*)(outp + (size_t)tok * DM + ncol) = o;
    if (!FINAL) {
      float f[8];
#pragma unroll
      for (int j = 0; j < 4; ++j) { f[2 * j] = bf_lo(o[j]); f[2 * j + 1] = bf_hi(o[j]); }
      u32x4 yv;
      yv[0] = pack2(f[0] * c0[0], f[1] * c0[1]); yv[1] = pack2(f[2] * c0[2], f[3] * c0[3]);
      yv[2] = pack2(f[4] * c1[0], f[5] * c1[1]); yv[3] = pack2(f[6] * c1[2], f[7] * c1[3]);
      *(u32x4*)(ws + WS_Y + ((size_t)((tok >> 4) * 32 + (ncol >> 5)) * 64 + (ech & 3) * 16 + (tok & 15)) * 16) = yv;
      float s = ((f[0] + f[1]) + (f[2] + f[3])) + ((f[4] + f[5]) + (f[6] + f[7]));
      s += __shfl_xor(s, 1); s += __shfl_xor(s, 2); s += __shfl_xor(s, 4);
      const float mu = s * (1.0f / 64.0f);
      float q2 = 0;
#pragma unroll
      for (int j = 0; j < 8; ++j) q2 += (f[j] - mu) * (f[j] - mu);
      q2 += __shfl_xor(q2, 1); q2 += __shfl_xor(q2, 2); q2 += __shfl_xor(q2, 4);
      if (ech == 0) { f32x2 st = {mu, q2}; *(f32x2*)(ws + WS_ST2 + ((size_t)tok * 16 + tn) * 8) = st; }
    }
  }
}

constexpr int K5_WOFF = 16384, K5_UOFF = 7168 + 16, K5_SLOT = 16384 + 7168 + 16 + 7168, K5_R = 5;
constexpr int K5_ST = K5_R * K5_SLOT;
constexpr int K5_UV = K5_ST + 2048;
constexpr int K5_LDS = K5_UV + 4 * 112 * 4;
__global__ __launch_bounds__(512, 2) void k5_gateup(const u16* __restrict__ wg, const u16* __restrict__ wu,
                                                    char* __restrict__ ws) {
  extern __shared__ __attribute__((aligned(16))) char smem_raw[];
  lptr lds = (lptr)smem_raw;
  const unsigned lds0 = (unsigned)(uintptr_t)lds;
  const int tid = threadIdx.x, lane = tid & 63;
  const int w = __builtin_amdgcn_readfirstlane(tid >> 6);
  const int xcd = blockIdx.x & 7, loc = blockIdx.x >> 3;
  const int tm = (xcd & 1) * 4 + (loc & 3);
  const int tn = (xcd >> 1) * 8 + (loc >> 2);
  const int mq = w >> 1, nh = w & 1;
  const u32x4 srdY = make_srd(ws + WS_Y, SEQ * DM * 2);
  const u32x4 srdG = make_srd(wg, DM * FF * 2);
  const u32x4 srdU = make_srd(wu, DM * FF * 2);
  const unsigned vlin = lane * 16;
  const int wi = (w < 7) ? w : 6;
  unsigned vW;
  {
    const int cidx = wi * 64 + lane, rho = cidx / 14, cc = cidx - rho * 14;
    const int kk = 8 * ((rho >> 2) & 3) + 4 * (rho >> 4) + (rho & 3);
    vW = kk * (FF * 2) + cc * 16;
  }
  const unsigned sT0 = (unsigned)(tm * 16 + 2 * w) * 32768, sW0 = tn * 224;
  const unsigned dT = w * 2048, dW = K5_WOFF + wi * 1024;
#define K5_STAGE(s, slotb) do { const unsigned b_ = lds0 + (slotb); \
    dma16(b_ + dT, vlin, srdY, sT0 + (s) * 1024); dma16(b_ + dT + 1024, vlin, srdY, sT0 + 32768 + (s) * 1024); \
    dma16(b_ + dW, vW, srdG, sW0 + (s) * (32 * FF * 2)); dma16(b_ + dW + K5_UOFF, vW, srdU, sW0 + (s) * (32 * FF * 2)); } while (0)
  K5_STAGE(0, 0);
  K5_STAGE(1, K5_SLOT);
  K5_STAGE(2, 2 * K5_SLOT);
  K5_STAGE(3, 3 * K5_SLOT);
  if (tid < 256) {
    const f32x4* p = (const f32x4*)(ws + WS_ST2 + (size_t)(tm * 256 + tid) * 128);
    f32x4 v[8];
#pragma unroll
    for (int i = 0; i < 8; ++i) v[i] = p[i];
    float ms = 0, m2 = 0;
#pragma unroll
    for (int i = 0; i < 8; ++i) { ms += v[i][0] + v[i][2]; m2 += v[i][1] + v[i][3]; }
    const float mean = ms * (1.0f / 16.0f);
    float d2 = 0;
#pragma unroll
    for (int i = 0; i < 8; ++i) d2 += (v[i][0] - mean) * (v[i][0] - mean) + (v[i][2] - mean) * (v[i][2] - mean);
    const float var = (m2 + 64.0f * d2) * (1.0f / DM);
    f32x2 st = {mean, 1.0f / sqrtf(var + 1e-5f)};
    *(LAS f32x2*)(lds + K5_ST + tid * 8) = st;
  } else if (tid < 256 + 224) {
    const int i = tid - 256, mat = i / 112, f = i - mat * 112;
    const float* uv = (const float*)(ws + WS_UV);
    const int col = mat * FF + tn * 112 + f;
    const float u = uv[col] + uv[2 * (2 * FF) + col], v = uv[2 * FF + col] + uv[3 * (2 * FF) + col];
    *(LAS float*)(lds + K5_UV + ((mat * 2 + 0) * 112 + f) * 4) = u;
    *(LAS float*)(lds + K5_UV + ((mat * 2 + 1) * 112 + f) * 4) = v;
  }
  f32x4 acc[7][4];
#pragma unroll
  for (int t = 0; t < 7; ++t)
#pragma unroll
    for (int b = 0; b < 4; ++b) acc[t][b] = (f32x4){0, 0, 0, 0};
  const int g = lane >> 4, q = (lane & 15) >> 2, p = lane & 3;
  const unsigned aoff = K5_WOFF + (p & 1) * K5_UOFF + (4 * g + q) * 224 + (56 * nh + 4 * (p >> 1)) * 2;
  const unsigned boff = mq * 4096 + lane * 16;
  bf16x8 A[7], B0[4], B1[4];
  WAIT_BAR(12);
#pragma unroll
  for (int t = 0; t < 7; ++t) A[t] = lds_tr2(lds + aoff + t * 16, lds + aoff + t * 16 + 3584);
#pragma unroll
  for (int b = 0; b < 4; ++b) B0[b] = lds_frag(lds + boff + b * 1024);
  int rd = K5_SLOT, wr = 4 * K5_SLOT;
#define K5_BAR(n) asm volatile("s_waitcnt vmcnt(" #n ") lgkmcnt(15)\n\ts_barrier" ::: "memory")
#define K5_STEP(s, BC, BN, DO_READ, DO_DMA) do { \
    lptr ra_ = lds + rd + aoff; lptr rb_ = lds + rd + boff; \
    if (DO_DMA) K5_STAGE((s) + 4, wr); \
    if (DO_READ) { _Pragma("unroll") for (int b = 0; b < 4; ++b) BN[b] = lds_frag(rb_ + b * 1024); } \
    _Pragma("unroll") for (int t = 0; t < 7; ++t) { \
      _Pragma("unroll") for (int b = 0; b < 4; ++b) acc[t][b] = MFMA16(A[t], BC[b], acc[t][b]); \
      if (DO_READ) A[t] = lds_tr2(ra_ + t * 16, ra_ + t * 16 + 3584); } \
    rd = (rd == 4 * K5_SLOT) ? 0 : rd + K5_SLOT; wr = (wr == 4 * K5_SLOT) ? 0 : wr + K5_SLOT; } while (0)
  for (int s = 0; s < 28; s += 2) {
    K5_BAR(8); K5_STEP(s, B0, B1, true, true);
    K5_BAR(8); K5_STEP(s + 1, B1, B0, true, true);
  }
  WAIT_BAR(8); K5_STEP(28, B0, B1, true, false);
  WAIT_BAR(4); K5_STEP(29, B1, B0, true, false);
  WAIT_BAR(0); K5_STEP(30, B0, B1, true, false);
  K5_STEP(31, B1, B0, false, false);
  {
    const int G = lane >> 4, cidx = lane & 15, hq = G >> 1;
#pragma unroll
    for (int P = 0; P < 2; ++P) {
      const int tl = mq * 64 + P * 32 + (G & 1) * 16 + cidx, tok = tm * 256 + tl;
      const f32x2 st = *(LAS f32x2*)(lds + K5_ST + tl * 8);
      const float rs = st[1], nm = -st[0] * st[1];
      char* drow = ws + WS_ACT + ((size_t)(tok >> 4) * 112 * 64 + hq * 16 + (tok & 15)) * 16;
      const int tile0 = 14 * tn + 7 * nh;
      u32x2 hv[7];
#pragma unroll
      for (int t = 0; t < 7; ++t) {
        const int fl = 56 * nh + 8 * t + 4 * hq;
        const f32x4 ug = *(LAS f32x4*)(lds + K5_UV + (0 * 112 + fl) * 4), vg = *(LAS f32x4*)(lds + K5_UV + (1 * 112 + fl) * 4);
        const f32x4 uu = *(LAS f32x4*)(lds + K5_UV + (2 * 112 + fl) * 4), vu = *(LAS f32x4*)(lds + K5_UV + (3 * 112 + fl) * 4);
        float a[4];
#pragma unroll
        for (int r = 0; r < 4; ++r) {
          auto sw = __builtin_amdgcn_permlane16_swap(__float_as_uint(acc[t][2 * P][r]), __float_as_uint(acc[t][2 * P + 1][r]), false, false);
          const float gt = rs * __uint_as_float(sw[0]) + (nm * ug[r] + vg[r]);
          const float up = rs * __uint_as_float(sw[1]) + (nm * uu[r] + vu[r]);
          a[r] = (gt > 0.0f ? gt : gt * 0.0f) * up;
        }
        hv[t] = (u32x2){pack2(a[0], a[1]), pack2(a[2], a[3])};
      }
      if (nh == 0) {
#pragma unroll
        for (int t = 0; t < 6; t += 2) {
          const int Bk = (tile0 + t) >> 1;
          *(u32x4*)(drow + ((Bk >> 1) * 64 + (Bk & 1) * 32) * 16) = (u32x4){hv[t][0], hv[t][1], hv[t + 1][0], hv[t + 1][1]};
        }
        const int Bk = (tile0 + 6) >> 1;
        *(u32x2*)(drow + ((Bk >> 1) * 64 + (Bk & 1) * 32) * 16) = hv[6];
      } else {
        const int Bk0 = tile0 >> 1;
        *(u32x2*)(drow + ((Bk0 >> 1) * 64 + (Bk0 & 1) * 32) * 16 + 8) = hv[0];
#pragma unroll
        for (int t = 1; t < 7; t += 2) {
          const int Bk = (tile0 + t) >> 1;
          *(u32x4*)(drow + ((Bk >> 1) * 64 + (Bk & 1) * 32) * 16) = (u32x4){hv[t][0], hv[t][1], hv[t + 1][0], hv[t + 1][1]};
        }
      }
    }
  }
}

extern "C" void kernel_launch(void* const* d_in, const int* in_sizes, int n_in, void* d_out, int out_size,
                              void* d_ws, size_t ws_size, hipStream_t stream) {
  const u16* x = (const u16*)d_in[0];
  const u16* wq = (const u16*)d_in[1];
  const u16* wk = (const u16*)d_in[2];
  const u16* wv = (const u16*)d_in[3];
  const u16* wo = (const u16*)d_in[4];
  const u16* wg = (const u16*)d_in[6];
  const u16* wu = (const u16*)d_in[7];
  const u16* wd = (const u16*)d_in[8];
  const u16* c = (const u16*)d_in[9];
  const u16* wmod = (const u16*)d_in[10];
  char* ws = (char*)d_ws;
  u16* out = (u16*)d_out;
  hipFuncSetAttribute((const void*)k3_attn, hipFuncAttributeMaxDynamicSharedMemorySize, K3_LDS);
  hipFuncSetAttribute((const void*)kg_gemm<DM, false>, hipFuncAttributeMaxDynamicSharedMemorySize, KG_LDS);
  hipFuncSetAttribute((const void*)kg_gemm<FF, true>, hipFuncAttributeMaxDynamicSharedMemorySize, KG_LDS);
  hipFuncSetAttribute((const void*)k5_gateup, hipFuncAttributeMaxDynamicSharedMemorySize, K5_LDS);
  k1_pre<<<K1_MOD_BLOCKS + K1_ST_BLOCKS + K1_WKV_BLOCKS, 256, 0, stream>>>(c, wmod, x, wk, wv, ws);
  k2_ln_kv<<<64, 512, 0, stream>>>(x, ws);
  k2b_uv<<<224, 256, 0, stream>>>(wg, wu, ws);
  k3_attn<<<256, 512, K3_LDS, stream>>>(wq, ws);
  kg_gemm<DM, false><<<256, 512, KG_LDS, stream>>>(ws + WS_O, wo, x, (u16*)(ws + WS_X1), ws);
  k5_gateup<<<256, 512, K5_LDS, stream>>>(wg, wu, ws);
  kg_gemm<FF, true><<<256, 512, KG_LDS, stream>>>(ws + WS_ACT, wd, (const u16*)(ws + WS_X1), out, ws);
}
```

```cpp
#include <hip/hip_runtime.h>
#include <hip/hip_bf16.h>
#include <stdint.h>

typedef unsigned short u16;
typedef __bf16 bf16x8 __attribute__((ext_vector_type(8)));
typedef __bf16 bf16x16 __attribute__((ext_vector_type(16)));
typedef short s16x4 __attribute__((ext_vector_type(4)));
typedef short s16x8 __attribute__((ext_vector_type(8)));
typedef float f32x2 __attribute__((ext_vector_type(2)));
typedef float f32x4 __attribute__((ext_vector_type(4)));
typedef float f32x8 __attribute__((ext_vector_type(8)));
typedef float f32x16 __attribute__((ext_vector_type(16)));
typedef unsigned u32x2 __attribute__((ext_vector_type(2)));
typedef unsigned u32x4 __attribute__((ext_vector_type(4)));
typedef unsigned u32x8 __attribute__((ext_vector_type(8)));
typedef unsigned u32x16 __attribute__((ext_vector_type(16)));
#define LAS __attribute__((address_space(3)))
typedef LAS char* lptr;

constexpr int SEQ = 2048, DM = 1024, FF = 3584, NMOD = 6144;
constexpr float QSCALE = 0.17677669529663687f * 1.4426950408889634f;
constexpr int MOD_KS = 4;

constexpr size_t WS_MODP = 0;
constexpr size_t WS_KST = WS_MODP + MOD_KS * NMOD * 4;
constexpr size_t WS_UV = WS_KST + 1024;
constexpr size_t WS_ST2 = WS_UV + 2 * 2 * 2 * FF * 4;
constexpr size_t WS_KV = WS_ST2 + SEQ * 16 * 8;
constexpr size_t WS_FLG = WS_KV + 65 * 4096;
constexpr int FL_EPOCH = 0, FL_1 = 256, FL_H = FL_1 + 8 * 256, FL_K = FL_H + 256, FL_3 = FL_K + 256, FL_U = FL_3 + 256, FL_A = FL_U + 256, FL_O = FL_A + 256,
              FL_G = FL_O + 256, FL_D = FL_G + 256, FL_END = FL_D + 256 * 16;
constexpr size_t WS_H = WS_FLG + FL_END * 8;
constexpr size_t WS_O = WS_H + (size_t)SEQ * DM * 2;
constexpr size_t WS_Y = WS_O + (size_t)SEQ * DM * 2;
constexpr size_t WS_ACT = WS_Y + (size_t)SEQ * DM * 2;
constexpr size_t WS_XD = WS_ACT + (size_t)SEQ * FF * 2;
constexpr size_t WS_END = WS_XD + 256 * 32768;
static_assert(WS_H % 1024 == 0, "alignment");

__device__ __forceinline__ float bf_lo(unsigned u) { return __uint_as_float(u << 16); }
__device__ __forceinline__ float bf_hi(unsigned u) { return __uint_as_float(u & 0xffff0000u); }
__device__ __forceinline__ unsigned pack2(float lo, float hi) {
  typedef __bf16 bf16x2 __attribute__((ext_vector_type(2)));
  f32x2 v = {lo, hi};
  bf16x2 r = __builtin_convertvector(v, bf16x2);
  return __builtin_bit_cast(unsigned, r);
}
__device__ __forceinline__ float rbf(float x) { return (float)(__bf16)x; }
__device__ __forceinline__ u32x4 make_srd(const void* p, unsigned bytes) {
  uint64_t a = (uint64_t)p;
  u32x4 r;
  r[0] = __builtin_amdgcn_readfirstlane((unsigned)a); r[1] = __builtin_amdgcn_readfirstlane((unsigned)(a >> 32));
  r[2] = __builtin_amdgcn_readfirstlane(bytes); r[3] = 0x00020000u;
  return r;
}
__device__ __forceinline__ void dma16(unsigned ldsaddr, unsigned voff, u32x4 srd, unsigned soff) {
  asm volatile("s_mov_b32 m0, %0\n\ts_nop 0\n\tbuffer_load_dwordx4 %1, %2, %3 offen lds"
               :: "s"(ldsaddr), "v"(voff), "s"(srd), "s"(soff) : "memory");
}
__device__ __forceinline__ void dma16s(unsigned ldsaddr, unsigned voff, u32x4 srd, unsigned soff) {
  asm volatile("s_mov_b32 m0, %0\n\ts_nop 0\n\tbuffer_load_dwordx4 %1, %2, %3 offen sc1 lds"
               :: "s"(ldsaddr), "v"(voff), "s"(srd), "s"(soff) : "memory");
}
typedef __amdgpu_buffer_rsrc_t rsrc_t;
__device__ __forceinline__ rsrc_t make_rsrc(void* p, unsigned bytes) { return __builtin_amdgcn_make_buffer_rsrc(p, 0, bytes, 0x00020000); }
#define OUT_AUX 0
#define ST_AUX 16
__device__ __forceinline__ void bstore4(rsrc_t r, unsigned voff, unsigned soff, u32x4 v) { __builtin_amdgcn_raw_buffer_store_b128(v, r, voff, soff, ST_AUX); }
__device__ __forceinline__ void bstore2(rsrc_t r, unsigned voff, unsigned soff, u32x2 v) { __builtin_amdgcn_raw_buffer_store_b64(v, r, voff, soff, ST_AUX); }
#define LGKM0_BAR() asm volatile("s_waitcnt lgkmcnt(0)\n\ts_barrier" ::: "memory")
__device__ __forceinline__ bf16x8 lds_frag(lptr p) { return *(LAS bf16x8*)p; }
__device__ __forceinline__ bf16x8 lds_tr2(lptr p0, lptr p1) {
  s16x4 a = __builtin_amdgcn_ds_read_tr16_b64_v4i16((LAS s16x4*)p0);
  s16x4 b = __builtin_amdgcn_ds_read_tr16_b64_v4i16((LAS s16x4*)p1);
  s16x8 r = __builtin_shufflevector(a, b, 0, 1, 2, 3, 4, 5, 6, 7);
  return __builtin_bit_cast(bf16x8, r);
}
#define MFMA16(a, b, c) __builtin_amdgcn_mfma_f32_16x16x32_bf16(a, b, c, 0, 0, 0)
#define MFMA32(a, b, c) __builtin_amdgcn_mfma_f32_32x32x16_bf16(a, b, c, 0, 0, 0)
__device__ __forceinline__ float swap32_sum(float v) {
  auto r = __builtin_amdgcn_permlane32_swap(__float_as_uint(v), __float_as_uint(v), false, false);
  return __uint_as_float(r[0]) + __uint_as_float(r[1]);
}
__device__ __forceinline__ float swap16_sum(float v) {
  auto r = __builtin_amdgcn_permlane16_swap(__float_as_uint(v), __float_as_uint(v), false, false);
  return __uint_as_float(r[0]) + __uint_as_float(r[1]);
}
__device__ __forceinline__ float swap32_max(float v) {
  auto r = __builtin_amdgcn_permlane32_swap(__float_as_uint(v), __float_as_uint(v), false, false);
  return fmaxf(__uint_as_float(r[0]), __uint_as_float(r[1]));
}
__device__ __forceinline__ float wave_max_pos(float f) {
  unsigned v = __float_as_uint(f);
#define DPPU(v, ctl) (unsigned)__builtin_amdgcn_mov_dpp((int)(v), ctl, 0xF, 0xF, true)
  v = max(v, DPPU(v, 0xB1)); v = max(v, DPPU(v, 0x4E)); v = max(v, DPPU(v, 0x141)); v = max(v, DPPU(v, 0x140));
  auto r = __builtin_amdgcn_permlane16_swap(v, v, false, false);
  v = max((unsigned)r[0], (unsigned)r[1]);
  auto q = __builtin_amdgcn_permlane32_swap(v, v, false, false);
  return __uint_as_float(max((unsigned)q[0], (unsigned)q[1]));
}
#define DPPF(v, ctl) __int_as_float(__builtin_amdgcn_mov_dpp(__float_as_int(v), ctl, 0xF, 0xF, true))
__device__ __forceinline__ float quad_sum(float v) { v += DPPF(v, 0xB1); v += DPPF(v, 0x4E); return v; }
__device__ __forceinline__ void bstore1(rsrc_t r, unsigned voff, unsigned soff, unsigned v) { __builtin_amdgcn_raw_buffer_store_b32(v, r, voff, soff, ST_AUX); }
__device__ __forceinline__ f32x4 bload4f(rsrc_t r, unsigned voff, unsigned soff) {
  return __builtin_bit_cast(f32x4, __builtin_amdgcn_raw_buffer_load_b128(r, voff, soff, 16));
}
typedef unsigned long long u64;
typedef __attribute__((address_space(1))) u64 gu64;
#define RLX_AGENT __ATOMIC_RELAXED, __HIP_MEMORY_SCOPE_AGENT
#define DRAIN_BAR() asm volatile("s_waitcnt vmcnt(0) lgkmcnt(0)\n\ts_barrier" ::: "memory")
#define BAR() asm volatile("s_barrier" ::: "memory")
#define EARLY(x) asm volatile("" : "+v"(x))
__device__ __forceinline__ void poll1(u32x4 srd, unsigned voff, unsigned soff, u64 tag) {
  u64 f;
  unsigned n = 0x10000;
  asm volatile("1:\n\tbuffer_load_dwordx2 %0, %2, %3, %4 offen sc1\n\ts_waitcnt vmcnt(0)\n\tv_cmp_ne_u64 vcc, %5, %0\n\ts_sub_u32 %1, %1, 1\n\t"
               "s_cbranch_vccz 2f\n\ts_cbranch_scc0 1b\n2:"
               : "=&v"(f), "+s"(n) : "v"(voff), "s"(srd), "s"(soff), "s"(tag) : "memory", "scc", "vcc");
}
__device__ __forceinline__ void poll4(u32x4 srd, unsigned voff, unsigned soff, u64 tag) {
  u64 f0, f1, f2, f3, m;
  unsigned n = 0x10000;
  asm volatile("1:\n\tbuffer_load_dwordx2 %0, %6, %7, %8 offen sc1\n\tbuffer_load_dwordx2 %1, %6, %7, %8 offen offset:512 sc1\n\t"
               "buffer_load_dwordx2 %2, %6, %7, %8 offen offset:1024 sc1\n\tbuffer_load_dwordx2 %3, %6, %7, %8 offen offset:1536 sc1\n\t"
               "s_waitcnt vmcnt(0)\n\tv_cmp_ne_u64 vcc, %9, %0\n\tv_cmp_ne_u64 %4, %9, %1\n\ts_or_b64 vcc, vcc, %4\n\t"
               "v_cmp_ne_u64 %4, %9, %2\n\ts_or_b64 vcc, vcc, %4\n\tv_cmp_ne_u64 %4, %9, %3\n\ts_or_b64 vcc, vcc, %4\n\t"
               "s_sub_u32 %5, %5, 1\n\ts_cbranch_vccz 2f\n\ts_cbranch_scc0 1b\n2:"
               : "=&v"(f0), "=&v"(f1), "=&v"(f2), "=&v"(f3), "=&s"(m), "+s"(n) : "v"(voff), "s"(srd), "s"(soff), "s"(tag) : "memory", "scc", "vcc");
}
__device__ __forceinline__ void put_flag(u32x4 srd, unsigned soff, u64 tag) {
  asm volatile("s_mov_b64 exec, 1\n\tbuffer_store_dwordx2 %0, off, %1, %2 sc1\n\ts_mov_b64 exec, -1" :: "v"(tag), "s"(srd), "s"(soff) : "memory");
}
__device__ __forceinline__ void mod_spread(f32x4 v, float one, u32x2 (&o)[4]) {
  const unsigned r0 = pack2(quad_sum(v[0]), quad_sum(v[1])), r1 = pack2(quad_sum(v[2]), quad_sum(v[3]));
  const unsigned p0 = pack2(bf_lo(r0) + one, bf_hi(r0) + one), p1 = pack2(bf_lo(r1) + one, bf_hi(r1) + one);
  o[0][0] = __builtin_amdgcn_mov_dpp(p0, 0x150, 0xF, 0xF, true); o[0][1] = __builtin_amdgcn_mov_dpp(p1, 0x150, 0xF, 0xF, true);
  o[1][0] = __builtin_amdgcn_mov_dpp(p0, 0x154, 0xF, 0xF, true); o[1][1] = __builtin_amdgcn_mov_dpp(p1, 0x154, 0xF, 0xF, true);
  o[2][0] = __builtin_amdgcn_mov_dpp(p0, 0x158, 0xF, 0xF, true); o[2][1] = __builtin_amdgcn_mov_dpp(p1, 0x158, 0xF, 0xF, true);
  o[3][0] = __builtin_amdgcn_mov_dpp(p0, 0x15C, 0xF, 0xF, true); o[3][1] = __builtin_amdgcn_mov_dpp(p1, 0x15C, 0xF, 0xF, true);
}
__device__ __forceinline__ void gemv_rows(u32x4 a, u32x4 b, float sa, float sb, LAS float* red, int lane) {
  float acc[8];
#pragma unroll
  for (int j = 0; j < 4; ++j) {
    acc[2 * j] = sa * bf_lo(a[j]) + sb * bf_lo(b[j]);
    acc[2 * j + 1] = sa * bf_hi(a[j]) + sb * bf_hi(b[j]);
  }
  float t[4], u[2];
#pragma unroll
  for (int j = 0; j < 4; ++j) {
    auto r = __builtin_amdgcn_permlane32_swap(__float_as_uint(acc[j]), __float_as_uint(acc[j + 4]), false, false);
    t[j] = __uint_as_float(r[0]) + __uint_as_float(r[1]);
  }
#pragma unroll
  for (int j = 0; j < 2; ++j) {
    auto r = __builtin_amdgcn_permlane16_swap(__float_as_uint(t[j]), __float_as_uint(t[j + 2]), false, false);
    u[j] = __uint_as_float(r[0]) + __uint_as_float(r[1]);
    u[j] += DPPF(u[j], 0x124);
    u[j] += DPPF(u[j], 0x128);
  }
  if ((lane & 12) == 0) *(LAS f32x2*)(red + (lane & 3) * 8 + (lane >> 4) * 2) = (f32x2){u[0], u[1]};
}
__device__ __forceinline__ float wave_sum2(float a, float b) {
  auto r = __builtin_amdgcn_permlane32_swap(__float_as_uint(a), __float_as_uint(b), false, false);
  float v = __uint_as_float(r[0]) + __uint_as_float(r[1]);
  v += DPPF(v, 0xB1); v += DPPF(v, 0x4E); v += DPPF(v, 0x141); v += DPPF(v, 0x140);
  return swap16_sum(v);
}
__device__ __forceinline__ float silu_bf(unsigned raw) {
  const float v = bf_lo(raw);
  return rbf(v * __builtin_amdgcn_rcpf(1.0f + __builtin_amdgcn_exp2f(-1.4426950408889634f * v)));
}

#define CLOB_240_255 "v240", "v241", "v242", "v243", "v244", "v245", "v246", "v247", "v248", "v249", "v250", "v251", "v252", "v253", "v254", "v255"
#define CLOB_64_95 "v64", "v65", "v66", "v67", "v68", "v69", "v70", "v71", "v72", "v73", "v74", "v75", "v76", "v77", "v78", "v79", "v80", "v81", "v82", "v83", "v84", "v85", "v86", "v87", "v88", "v89", "v90", "v91", "v92", "v93", "v94", "v95"
#define CLOB_96_159 "v96", "v97", "v98", "v99", "v100", "v101", "v102", "v103", "v104", "v105", "v106", "v107", "v108", "v109", "v110", "v111", "v112", "v113", "v114", "v115", "v116", "v117", "v118", "v119", "v120", "v121", "v122", "v123", "v124", "v125", "v126", "v127", "v128", "v129", "v130", "v131", "v132", "v133", "v134", "v135", "v136", "v137", "v138", "v139", "v140", "v141", "v142", "v143", "v144", "v145", "v146", "v147", "v148", "v149", "v150", "v151", "v152", "v153", "v154", "v155", "v156", "v157", "v158", "v159"
#define CLOB_64_71 "v64", "v65", "v66", "v67", "v68", "v69", "v70", "v71"
#define CLOB_144_223 "v144", "v145", "v146", "v147", "v148", "v149", "v150", "v151", "v152", "v153", "v154", "v155", "v156", "v157", "v158", "v159", "v160", "v161", "v162", "v163", "v164", "v165", "v166", "v167", "v168", "v169", "v170", "v171", "v172", "v173", "v174", "v175", "v176", "v177", "v178", "v179", "v180", "v181", "v182", "v183", "v184", "v185", "v186", "v187", "v188", "v189", "v190", "v191", "v192", "v193", "v194", "v195", "v196", "v197", "v198", "v199", "v200", "v201", "v202", "v203", "v204", "v205", "v206", "v207", "v208", "v209", "v210", "v211", "v212", "v213", "v214", "v215", "v216", "v217", "v218", "v219", "v220", "v221", "v222", "v223"
#define CLOB_160_215 "v160", "v161", "v162", "v163", "v164", "v165", "v166", "v167", "v168", "v169", "v170", "v171", "v172", "v173", "v174", "v175", "v176", "v177", "v178", "v179", "v180", "v181", "v182", "v183", "v184", "v185", "v186", "v187", "v188", "v189", "v190", "v191", "v192", "v193", "v194", "v195", "v196", "v197", "v198", "v199", "v200", "v201", "v202", "v203", "v204", "v205", "v206", "v207", "v208", "v209", "v210", "v211", "v212", "v213", "v214", "v215"
#define CLOB_G "v14", "v15", "v32", "v33", "v34", "v35", "v36", "v37", "v38", "v39", "v40", "v41", "v42", "v43", "v44", "v45", "v46", "v47", "v48", "v49", "v50", "v51", "v52", "v53", "v54", "v55", "v56", "v57", "v58", "v59", "v60", "v61", "v62", "v63", "v64", "v65", "v66", "v67", "v68", "v69", "v70", "v71", "v72", "v73", "v74", "v75", "v76", "v77", "v78", "v79", "v80", "v81", "v82", "v83", "v84", "v85", "v86", "v87", "v88", "v89", "v90", "v91", "v92", "v93", "v94", "v95", "v96", "v97", "v98", "v99", "v100", "v101", "v102", "v103", "v104", "v105", "v106", "v107", "v108", "v109", "v110", "v111", "v112", "v113", "v114", "v115", "v116", "v117", "v118", "v119", "v120", "v121", "v122", "v123", "v124", "v125", "v126", "v127", "v128", "v129", "v130", "v131", "v132", "v133", "v134", "v135", "v136", "v137", "v138", "v139", "v140", "v141", "v142", "v143", "v144", "v145", "v146", "v147", "v148", "v149", "v150", "v151", "v152", "v153", "v154", "v155", "v156", "v157", "v158", "v159", "v160", "v161", "v162", "v163", "v164", "v165", "v166", "v167", "v168", "v169", "v170", "v171", "v172", "v173", "v174", "v175", "v176", "v177", "v178", "v179", "v180", "v181", "v182", "v183", "v184", "v185", "v186", "v187", "v188", "v189", "v190", "v191", "v192", "v193", "v194", "v195", "v196", "v197", "v198", "v199", "v200", "v201", "v202", "v203", "v204", "v205", "v206", "v207", "v208", "v209", "v210", "v211", "v212", "v213", "v214", "v215", "v216", "v217", "v218", "v219", "v220", "v221", "v222", "v223", "v224", "v225", "v226", "v227", "v228", "v229", "v230", "v231", "v232", "v233", "v234", "v235", "v236", "v237", "v238", "v239", "v242", "v243", "v244", "v245", "v246", "v247", "v248", "v249", "v250", "v251", "v252", "v253", "v254", "v255"
#define CLOB_80_111 "v80", "v81", "v82", "v83", "v84", "v85", "v86", "v87", "v88", "v89", "v90", "v91", "v92", "v93", "v94", "v95", "v96", "v97", "v98", "v99", "v100", "v101", "v102", "v103", "v104", "v105", "v106", "v107", "v108", "v109", "v110", "v111"
#define K3_ASM_LOOP \
  "v_xor_b32 v80, 0x80000000, %[ma]\n\t" \
  "v_xor_b32 v96, 0x80000000, %[mb]\n\t" \
  "v_mov_b32 v81, v80\n\t" \
  "v_mov_b32 v97, v96\n\t" \
  "v_mov_b64 v[82:83], v[80:81]\n\t" \
  "v_mov_b64 v[98:99], v[96:97]\n\t" \
  "v_mov_b64 v[84:85], v[80:81]\n\t" \
  "v_mov_b64 v[100:101], v[96:97]\n\t" \
  "v_mov_b64 v[86:87], v[80:81]\n\t" \
  "v_mov_b64 v[102:103], v[96:97]\n\t" \
  "v_mov_b64 v[88:89], v[80:81]\n\t" \
  "v_mov_b64 v[104:105], v[96:97]\n\t" \
  "v_mov_b64 v[90:91], v[80:81]\n\t" \
  "v_mov_b64 v[106:107], v[96:97]\n\t" \
  "v_mov_b64 v[92:93], v[80:81]\n\t" \
  "v_mov_b64 v[108:109], v[96:97]\n\t" \
  "v_mov_b64 v[94:95], v[80:81]\n\t" \
  "v_mov_b64 v[110:111], v[96:97]\n\t" \
  "s_nop 1\n\t" \
  "v_mfma_f32_32x32x16_bf16 v[160:175], v[56:59], v[32:35], v[80:95]\n\t" \
  "v_mfma_f32_32x32x16_bf16 v[192:207], v[56:59], v[40:43], v[96:111]\n\t" \
  "v_mfma_f32_32x32x16_bf16 v[160:175], v[60:63], v[36:39], v[160:175]\n\t" \
  "v_mfma_f32_32x32x16_bf16 v[192:207], v[60:63], v[44:47], v[192:207]\n\t" \
  "ds_read_b128 v[68:71], %[ac] offset:3072\n\t" \
  "ds_read_b128 v[56:59], %[ac]\n\t" \
  "ds_read_b128 v[60:63], %[ac] offset:1024\n\t" \
  "ds_read_b128 v[64:67], %[ac] offset:2048\n\t" \
  "v_mov_b64 v[152:153], 0\n\t" \
  "v_mov_b64 v[154:155], 0\n\t" \
  "v_mov_b64 v[156:157], 0\n\t" \
  "v_mov_b64 v[158:159], 0\n\t" \
  "v_mov_b64 v[112:113], 0\n\t" \
  "v_mov_b64 v[114:115], 0\n\t" \
  "v_mov_b64 v[116:117], 0\n\t" \
  "v_mov_b64 v[118:119], 0\n\t" \
  "v_mov_b64 v[120:121], 0\n\t" \
  "v_mov_b64 v[122:123], 0\n\t" \
  "v_mov_b64 v[124:125], 0\n\t" \
  "v_mov_b64 v[126:127], 0\n\t" \
  "v_mov_b64 v[128:129], 0\n\t" \
  "v_mov_b64 v[130:131], 0\n\t" \
  "v_mov_b64 v[132:133], 0\n\t" \
  "v_mov_b64 v[134:135], 0\n\t" \
  "v_mov_b64 v[136:137], 0\n\t" \
  "v_mov_b64 v[138:139], 0\n\t" \
  "v_mov_b64 v[140:141], 0\n\t" \
  "v_mov_b64 v[142:143], 0\n\t" \
  "v_mov_b64 v[72:73], 0\n\t" \
  "v_mov_b64 v[74:75], 0\n\t" \
  "v_mov_b64 v[76:77], 0\n\t" \
  "v_mov_b64 v[78:79], 0\n\t" \
  "v_mov_b32 v52, 0xdddddddd\n\t" \
  "s_mov_b32 %[c], 8\n\t" \
  "s_mov_b64 s[84:85], %[srdK0]\n\t" \
  "s_mov_b32 s86, 0x40000\n\t" \
  "s_mov_b32 s87, 0x20000\n\t" \
  "s_mov_b32 s88, %[so2]\n\t" \
  "s_movk_i32 s89, 0x400\n\t" \
  "v_mov_b32 v55, %[vlin]\n\t" \
  "s_getpc_b64 s[90:91]\n\t" \
  ".Lk3pc_%=:\n\t" \
  "s_add_u32 s90, s90, .Lk3act0_%=-.Lk3pc_%=\n\t" \
  "s_addc_u32 s91, s91, 0\n\t" \
  "s_branch 1f\n\t" \
  ".Lk3act0_%=:\n\t" \
  "s_add_u32 m0, %[dk], 0x8000\n\t" \
  "s_nop 0\n\t" \
  "buffer_load_dwordx4 %[vlin], %[srdK], s88 offen offset:0 sc1 lds\n\t" \
  "buffer_load_dwordx4 %[vlin], %[srdK], s88 offen offset:1024 sc1 lds\n\t" \
  "buffer_load_dwordx4 %[vlin], %[srdK], s88 offen offset:2048 sc1 lds\n\t" \
  "buffer_load_dwordx4 %[vlin], %[srdK], s88 offen offset:3072 sc1 lds\n\t" \
  "s_add_u32 s88, s88, 0x4000\n\t" \
  "s_add_u32 s90, s90, .Lk3act1_%=-.Lk3act0_%=\n\t" \
  "s_addc_u32 s91, s91, 0\n\t" \
  "s_setpc_b64 s[92:93]\n\t" \
  ".Lk3act1_%=:\n\t" \
  "v_xor_b32 v224, 0, v1\n\t" \
  "v_xor_b32 v225, 32, v1\n\t" \
  "v_xor_b32 v226, 64, v1\n\t" \
  "v_xor_b32 v227, 96, v1\n\t" \
  "ds_read_b64_tr_b16 v[240:241], v224\n\t" \
  "ds_read_b64_tr_b16 v[242:243], v224 offset:2048\n\t" \
  "ds_read_b64_tr_b16 v[244:245], v225\n\t" \
  "ds_read_b64_tr_b16 v[246:247], v225 offset:2048\n\t" \
  "ds_read_b64_tr_b16 v[248:249], v226\n\t" \
  "ds_read_b64_tr_b16 v[250:251], v226 offset:2048\n\t" \
  "ds_read_b64_tr_b16 v[252:253], v227\n\t" \
  "ds_read_b64_tr_b16 v[254:255], v227 offset:2048\n\t" \
  "s_waitcnt lgkmcnt(0)\n\t" \
  "v_mfma_f32_16x16x32_bf16 v[224:227], v[240:243], v[4:7], 0\n\t" \
  "v_mfma_f32_16x16x32_bf16 v[228:231], v[244:247], v[4:7], 0\n\t" \
  "v_mfma_f32_16x16x32_bf16 v[232:235], v[248:251], v[4:7], 0\n\t" \
  "v_mfma_f32_16x16x32_bf16 v[236:239], v[252:255], v[4:7], 0\n\t" \
  "v_lshrrev_b32 v240, 8, %[vlin]\n\t" \
  "v_lshlrev_b32 v240, 4, v240\n\t" \
  "v_add_u32 v240, %[dk], v240\n\t" \
  "v_add_u32 v240, 0x20000, v240\n\t" \
  "s_mov_b32 exec_lo, 0x00010001\n\t" \
  "s_mov_b32 exec_hi, 0x00010001\n\t" \
  "s_nop 5\n\t" \
  "ds_write_b128 v240, v[224:227] offset:0\n\t" \
  "ds_write_b128 v240, v[228:231] offset:64\n\t" \
  "ds_write_b128 v240, v[232:235] offset:128\n\t" \
  "ds_write_b128 v240, v[236:239] offset:192\n\t" \
  "s_mov_b64 exec, -1\n\t" \
  "v_min_u32 v252, 432, %[vlin]\n\t" \
  "v_lshlrev_b32 v252, 3, v252\n\t" \
  "buffer_load_dword v53, v252, %[srdP], %[otch] offen\n\t" \
  "s_add_u32 s90, s90, .Lk3act2_%=-.Lk3act1_%=\n\t" \
  "s_addc_u32 s91, s91, 0\n\t" \
  "s_setpc_b64 s[92:93]\n\t" \
  ".Lk3act2_%=:\n\t" \
  "s_cmp_lg_u32 %[wv], 0\n\t" \
  "s_cbranch_scc1 7f\n\t" \
  "v_lshrrev_b32 v240, 2, %[vlin]\n\t" \
  "v_add_u32 v241, %[dk], v240\n\t" \
  "v_add_u32 v241, 0x20000, v241\n\t" \
  "ds_read_b32 v224, v241 offset:0\n\t" \
  "ds_read_b32 v225, v241 offset:4096\n\t" \
  "ds_read_b32 v226, v241 offset:8192\n\t" \
  "ds_read_b32 v227, v241 offset:12288\n\t" \
  "ds_read_b32 v228, v241 offset:16384\n\t" \
  "ds_read_b32 v229, v241 offset:20480\n\t" \
  "ds_read_b32 v230, v241 offset:24576\n\t" \
  "ds_read_b32 v231, v241 offset:28672\n\t" \
  "s_waitcnt lgkmcnt(0)\n\t" \
  "v_add_f32 v224, v224, v225\n\t" \
  "v_add_f32 v226, v226, v227\n\t" \
  "v_add_f32 v228, v228, v229\n\t" \
  "v_add_f32 v230, v230, v231\n\t" \
  "v_add_f32 v224, v224, v226\n\t" \
  "v_add_f32 v228, v228, v230\n\t" \
  "v_add_f32 v224, v224, v228\n\t" \
  "buffer_store_dword v224, v240, %[srdX], %[oF] offen sc1\n\t" \
  "7:\n\t" \
  "s_add_u32 s90, s90, .Lk3act3_%=-.Lk3act2_%=\n\t" \
  "s_addc_u32 s91, s91, 0\n\t" \
  "s_setpc_b64 s[92:93]\n\t" \
  ".Lk3act3_%=:\n\t" \
  "s_waitcnt vmcnt(4)\n\t" \
  "s_cmp_lg_u32 %[wv], 0\n\t" \
  "s_cbranch_scc1 7f\n\t" \
  "v_mov_b32 v232, %[tl]\n\t" \
  "v_mov_b32 v233, %[th]\n\t" \
  "buffer_store_dwordx2 v[232:233], off, %[srdX], %[o3] sc1\n\t" \
  "7:\n\t" \
  "s_add_u32 m0, %[dk], 0x20000\n\t" \
  "s_nop 0\n\t" \
  "buffer_load_dwordx4 v0, %[srdU], %[sU] offen lds\n\t" \
  "s_add_u32 %[sU], %[sU], %[st8]\n\t" \
  "s_add_u32 m0, m0, 0x400\n\t" \
  "s_nop 0\n\t" \
  "buffer_load_dwordx4 v0, %[srdU], %[sU] offen lds\n\t" \
  "s_add_u32 %[sU], %[sU], %[st8]\n\t" \
  "s_add_u32 m0, m0, 0x400\n\t" \
  "s_nop 0\n\t" \
  "buffer_load_dwordx4 v0, %[srdU], %[sU] offen lds\n\t" \
  "s_add_u32 %[sU], %[sU], %[st8]\n\t" \
  "s_add_u32 m0, m0, 0x400\n\t" \
  "s_nop 0\n\t" \
  "buffer_load_dwordx4 v0, %[srdU], %[sU] offen lds\n\t" \
  "s_add_u32 %[sU], %[sU], %[st8]\n\t" \
  "s_add_u32 s90, s90, .Lk3act4_%=-.Lk3act3_%=\n\t" \
  "s_addc_u32 s91, s91, 0\n\t" \
  "s_setpc_b64 s[92:93]\n\t" \
  ".Lk3act4_%=:\n\t" \
  "v_lshrrev_b32 v252, 1, %[vlin]\n\t" \
  "v_and_b32 v252, 0xff, v252\n\t" \
  "buffer_load_dwordx2 v[248:249], v252, %[srdX], %[of3] offen sc1\n\t" \
  "s_mov_b64 s[84:85], %[srdU0]\n\t" \
  "s_mov_b32 s86, %[lenU]\n\t" \
  "s_add_u32 s88, %[sU], %[sU2]\n\t" \
  "s_mov_b32 s89, %[st8]\n\t" \
  "v_mov_b32 v55, v0\n\t" \
  "s_add_u32 s90, s90, .Lk3act5_%=-.Lk3act4_%=\n\t" \
  "s_addc_u32 s91, s91, 0\n\t" \
  "s_setpc_b64 s[92:93]\n\t" \
  ".Lk3act5_%=:\n\t" \
  "s_waitcnt vmcnt(4)\n\t" \
  "v_lshrrev_b32 v252, 1, %[vlin]\n\t" \
  "v_and_b32 v252, 0xff, v252\n\t" \
  "v_mov_b32 v250, %[tl]\n\t" \
  "v_mov_b32 v251, %[th]\n\t" \
  "s_mov_b32 %[nx2], 0x8000\n\t" \
  "8:\n\t" \
  "v_cmp_ne_u64 vcc, v[248:249], v[250:251]\n\t" \
  "s_nop 4\n\t" \
  "s_cbranch_vccz 7f\n\t" \
  "s_sub_u32 %[nx2], %[nx2], 1\n\t" \
  "s_cbranch_scc1 7f\n\t" \
  "s_sleep 1\n\t" \
  "buffer_load_dwordx2 v[248:249], v252, %[srdX], %[of3] offen sc1\n\t" \
  "s_waitcnt vmcnt(0)\n\t" \
  "s_branch 8b\n\t" \
  "7:\n\t" \
  "s_mov_b32 s86, 0\n\t" \
  "s_add_u32 s90, s90, .Lk3act6_%=-.Lk3act5_%=\n\t" \
  "s_addc_u32 s91, s91, 0\n\t" \
  "s_setpc_b64 s[92:93]\n\t" \
  ".Lk3act6_%=:\n\t" \
  "buffer_load_dwordx4 v[224:227], v2, %[srdX], 0 offen sc1\n\t" \
  "buffer_load_dwordx4 v[228:231], v2, %[srdX], 0 offen offset:64 sc1\n\t" \
  "buffer_load_dwordx4 v[232:235], v3, %[srdX], 0 offen sc1\n\t" \
  "s_mov_b32 %[nx2], 0x2000\n\t" \
  "buffer_load_dwordx4 v[236:239], v3, %[srdX], %[nx2] offen sc1\n\t" \
  "s_add_u32 s90, s90, .Lk3act7_%=-.Lk3act6_%=\n\t" \
  "s_addc_u32 s91, s91, 0\n\t" \
  "s_setpc_b64 s[92:93]\n\t" \
  ".Lk3act7_%=:\n\t" \
  "s_setpc_b64 s[92:93]\n\t" \
  "1:\n\t" \
  "s_setprio 2\n\t" \
  "s_waitcnt lgkmcnt(2)\n\t" \
  "v_mfma_f32_32x32x16_bf16 v[176:191], v[56:59], v[32:35], v[80:95]\n\t" \
  "v_exp_f32 v144, v160\n\t" \
  "v_exp_f32 v145, v161\n\t" \
  "v_exp_f32 v146, v162\n\t" \
  "v_exp_f32 v147, v163\n\t" \
  "v_smfmac_f32_32x32x32_bf16 v[128:143], v[68:71], v[152:159], v52\n\t" \
  "ds_read_b128 v[68:71], %[ac] offset:3072\n\t" \
  "v_exp_f32 v148, v164\n\t" \
  "v_exp_f32 v149, v165\n\t" \
  "v_smfmac_f32_16x16x64_bf16 v[76:79], v[48:51], v[152:159], v52\n\t" \
  "v_exp_f32 v150, v166\n\t" \
  "v_exp_f32 v151, v167\n\t" \
  "s_waitcnt lgkmcnt(2)\n\t" \
  "v_mfma_f32_32x32x16_bf16 v[176:191], v[60:63], v[36:39], v[176:191]\n\t" \
  "v_exp_f32 v152, v168\n\t" \
  "v_exp_f32 v153, v169\n\t" \
  "v_exp_f32 v154, v170\n\t" \
  "v_exp_f32 v155, v171\n\t" \
  "s_waitcnt lgkmcnt(1)\n\t" \
  "v_smfmac_f32_32x32x32_bf16 v[112:127], v[64:67], v[144:151], v52\n\t" \
  "v_exp_f32 v156, v172\n\t" \
  "v_exp_f32 v157, v173\n\t" \
  "v_smfmac_f32_16x16x64_bf16 v[72:75], v[48:51], v[144:151], v52\n\t" \
  "v_exp_f32 v158, v174\n\t" \
  "v_exp_f32 v159, v175\n\t" \
  "v_mfma_f32_32x32x16_bf16 v[208:223], v[56:59], v[40:43], v[96:111]\n\t" \
  "ds_read_b128 v[56:59], %[ac] offset:4096\n\t" \
  "v_exp_f32 v144, v192\n\t" \
  "v_exp_f32 v145, v193\n\t" \
  "v_exp_f32 v146, v194\n\t" \
  "v_exp_f32 v147, v195\n\t" \
  "s_waitcnt lgkmcnt(1)\n\t" \
  "v_smfmac_f32_32x32x32_bf16 v[112:127], v[68:71], v[152:159], v52\n\t" \
  "v_exp_f32 v148, v196\n\t" \
  "v_exp_f32 v149, v197\n\t" \
  "v_smfmac_f32_16x16x64_bf16 v[72:75], v[48:51], v[152:159], v52\n\t" \
  "v_exp_f32 v150, v198\n\t" \
  "v_exp_f32 v151, v199\n\t" \
  "v_mfma_f32_32x32x16_bf16 v[208:223], v[60:63], v[44:47], v[208:223]\n\t" \
  "ds_read_b128 v[60:63], %[ac] offset:5120\n\t" \
  "v_exp_f32 v152, v200\n\t" \
  "v_exp_f32 v153, v201\n\t" \
  "v_exp_f32 v154, v202\n\t" \
  "v_exp_f32 v155, v203\n\t" \
  "v_smfmac_f32_32x32x32_bf16 v[128:143], v[64:67], v[144:151], v52\n\t" \
  "ds_read_b128 v[64:67], %[ac] offset:6144\n\t" \
  "v_exp_f32 v156, v204\n\t" \
  "v_exp_f32 v157, v205\n\t" \
  "v_smfmac_f32_16x16x64_bf16 v[76:79], v[48:51], v[144:151], v52\n\t" \
  "v_exp_f32 v158, v206\n\t" \
  "v_exp_f32 v159, v207\n\t" \
  "s_setprio 1\n\t" \
  "s_waitcnt lgkmcnt(2)\n\t" \
  "v_mfma_f32_32x32x16_bf16 v[160:175], v[56:59], v[32:35], v[80:95]\n\t" \
  "v_exp_f32 v144, v176\n\t" \
  "v_exp_f32 v145, v177\n\t" \
  "v_exp_f32 v146, v178\n\t" \
  "v_exp_f32 v147, v179\n\t" \
  "v_smfmac_f32_32x32x32_bf16 v[128:143], v[68:71], v[152:159], v52\n\t" \
  "ds_read_b128 v[68:71], %[ac] offset:7168\n\t" \
  "v_exp_f32 v148, v180\n\t" \
  "v_exp_f32 v149, v181\n\t" \
  "v_smfmac_f32_16x16x64_bf16 v[76:79], v[48:51], v[152:159], v52\n\t" \
  "v_exp_f32 v150, v182\n\t" \
  "v_exp_f32 v151, v183\n\t" \
  "s_waitcnt lgkmcnt(2)\n\t" \
  "v_mfma_f32_32x32x16_bf16 v[160:175], v[60:63], v[36:39], v[160:175]\n\t" \
  "v_exp_f32 v152, v184\n\t" \
  "v_exp_f32 v153, v185\n\t" \
  "v_exp_f32 v154, v186\n\t" \
  "v_exp_f32 v155, v187\n\t" \
  "s_waitcnt lgkmcnt(1)\n\t" \
  "v_smfmac_f32_32x32x32_bf16 v[112:127], v[64:67], v[144:151], v52\n\t" \
  "v_exp_f32 v156, v188\n\t" \
  "v_exp_f32 v157, v189\n\t" \
  "v_smfmac_f32_16x16x64_bf16 v[72:75], v[48:51], v[144:151], v52\n\t" \
  "v_exp_f32 v158, v190\n\t" \
  "v_exp_f32 v159, v191\n\t" \
  "v_mfma_f32_32x32x16_bf16 v[192:207], v[56:59], v[40:43], v[96:111]\n\t" \
  "ds_read_b128 v[56:59], %[ac] offset:8192\n\t" \
  "v_exp_f32 v144, v208\n\t" \
  "v_exp_f32 v145, v209\n\t" \
  "v_exp_f32 v146, v210\n\t" \
  "v_exp_f32 v147, v211\n\t" \
  "s_waitcnt lgkmcnt(1)\n\t" \
  "v_smfmac_f32_32x32x32_bf16 v[112:127], v[68:71], v[152:159], v52\n\t" \
  "v_exp_f32 v148, v212\n\t" \
  "v_exp_f32 v149, v213\n\t" \
  "v_smfmac_f32_16x16x64_bf16 v[72:75], v[48:51], v[152:159], v52\n\t" \
  "v_exp_f32 v150, v214\n\t" \
  "v_exp_f32 v151, v215\n\t" \
  "v_mfma_f32_32x32x16_bf16 v[192:207], v[60:63], v[44:47], v[192:207]\n\t" \
  "ds_read_b128 v[60:63], %[ac] offset:9216\n\t" \
  "v_exp_f32 v152, v216\n\t" \
  "v_exp_f32 v153, v217\n\t" \
  "v_exp_f32 v154, v218\n\t" \
  "v_exp_f32 v155, v219\n\t" \
  "v_smfmac_f32_32x32x32_bf16 v[128:143], v[64:67], v[144:151], v52\n\t" \
  "ds_read_b128 v[64:67], %[ac] offset:10240\n\t" \
  "v_exp_f32 v156, v220\n\t" \
  "v_exp_f32 v157, v221\n\t" \
  "v_smfmac_f32_16x16x64_bf16 v[76:79], v[48:51], v[144:151], v52\n\t" \
  "v_exp_f32 v158, v222\n\t" \
  "v_exp_f32 v159, v223\n\t" \
  "s_setprio 0\n\t" \
  "s_waitcnt lgkmcnt(2)\n\t" \
  "v_mfma_f32_32x32x16_bf16 v[176:191], v[56:59], v[32:35], v[80:95]\n\t" \
  "v_exp_f32 v144, v160\n\t" \
  "v_exp_f32 v145, v161\n\t" \
  "v_exp_f32 v146, v162\n\t" \
  "v_exp_f32 v147, v163\n\t" \
  "v_smfmac_f32_32x32x32_bf16 v[128:143], v[68:71], v[152:159], v52\n\t" \
  "ds_read_b128 v[68:71], %[ac] offset:11264\n\t" \
  "v_exp_f32 v148, v164\n\t" \
  "v_exp_f32 v149, v165\n\t" \
  "v_smfmac_f32_16x16x64_bf16 v[76:79], v[48:51], v[152:159], v52\n\t" \
  "v_exp_f32 v150, v166\n\t" \
  "v_exp_f32 v151, v167\n\t" \
  "s_waitcnt lgkmcnt(2)\n\t" \
  "v_mfma_f32_32x32x16_bf16 v[176:191], v[60:63], v[36:39], v[176:191]\n\t" \
  "v_exp_f32 v152, v168\n\t" \
  "v_exp_f32 v153, v169\n\t" \
  "v_exp_f32 v154, v170\n\t" \
  "v_exp_f32 v155, v171\n\t" \
  "s_waitcnt lgkmcnt(1)\n\t" \
  "v_smfmac_f32_32x32x32_bf16 v[112:127], v[64:67], v[144:151], v52\n\t" \
  "v_exp_f32 v156, v172\n\t" \
  "v_exp_f32 v157, v173\n\t" \
  "v_smfmac_f32_16x16x64_bf16 v[72:75], v[48:51], v[144:151], v52\n\t" \
  "v_exp_f32 v158, v174\n\t" \
  "v_exp_f32 v159, v175\n\t" \
  "v_mfma_f32_32x32x16_bf16 v[208:223], v[56:59], v[40:43], v[96:111]\n\t" \
  "ds_read_b128 v[56:59], %[ac] offset:12288\n\t" \
  "v_exp_f32 v144, v192\n\t" \
  "v_exp_f32 v145, v193\n\t" \
  "v_exp_f32 v146, v194\n\t" \
  "v_exp_f32 v147, v195\n\t" \
  "s_waitcnt lgkmcnt(1)\n\t" \
  "v_smfmac_f32_32x32x32_bf16 v[112:127], v[68:71], v[152:159], v52\n\t" \
  "v_exp_f32 v148, v196\n\t" \
  "v_exp_f32 v149, v197\n\t" \
  "v_smfmac_f32_16x16x64_bf16 v[72:75], v[48:51], v[152:159], v52\n\t" \
  "v_exp_f32 v150, v198\n\t" \
  "v_exp_f32 v151, v199\n\t" \
  "v_mfma_f32_32x32x16_bf16 v[208:223], v[60:63], v[44:47], v[208:223]\n\t" \
  "ds_read_b128 v[60:63], %[ac] offset:13312\n\t" \
  "v_exp_f32 v152, v200\n\t" \
  "v_exp_f32 v153, v201\n\t" \
  "v_exp_f32 v154, v202\n\t" \
  "v_exp_f32 v155, v203\n\t" \
  "v_smfmac_f32_32x32x32_bf16 v[128:143], v[64:67], v[144:151], v52\n\t" \
  "ds_read_b128 v[64:67], %[ac] offset:14336\n\t" \
  "v_exp_f32 v156, v204\n\t" \
  "v_exp_f32 v157, v205\n\t" \
  "v_smfmac_f32_16x16x64_bf16 v[76:79], v[48:51], v[144:151], v52\n\t" \
  "v_exp_f32 v158, v206\n\t" \
  "v_exp_f32 v159, v207\n\t" \
  "s_add_u32 %[nx], %[cs], 0x8000\n\t" \
  "s_and_b32 %[nx], %[nx], 0x1ffff\n\t" \
  "s_add_u32 %[nx2], %[nx], %[kb]\n\t" \
  "v_add_u32 %[an], %[nx2], %[vlin]\n\t" \
  "s_add_u32 m0, %[pv], %[dk]\n\t" \
  "s_waitcnt vmcnt(4)\n\t" \
  "s_barrier\n\t" \
  "s_setprio 3\n\t" \
  "s_waitcnt lgkmcnt(2)\n\t" \
  "v_mfma_f32_32x32x16_bf16 v[160:175], v[56:59], v[32:35], v[80:95]\n\t" \
  "v_exp_f32 v144, v176\n\t" \
  "v_exp_f32 v145, v177\n\t" \
  "v_exp_f32 v146, v178\n\t" \
  "v_exp_f32 v147, v179\n\t" \
  "v_smfmac_f32_32x32x32_bf16 v[128:143], v[68:71], v[152:159], v52\n\t" \
  "ds_read_b128 v[68:71], %[ac] offset:15360\n\t" \
  "v_exp_f32 v148, v180\n\t" \
  "v_exp_f32 v149, v181\n\t" \
  "v_smfmac_f32_16x16x64_bf16 v[76:79], v[48:51], v[152:159], v52\n\t" \
  "buffer_load_dwordx4 v55, s[84:87], s88 offen sc1 lds\n\t" \
  "s_add_u32 s88, s88, s89\n\t" \
  "s_add_u32 m0, m0, 0x400\n\t" \
  "v_exp_f32 v150, v182\n\t" \
  "v_exp_f32 v151, v183\n\t" \
  "s_waitcnt lgkmcnt(2)\n\t" \
  "v_mfma_f32_32x32x16_bf16 v[160:175], v[60:63], v[36:39], v[160:175]\n\t" \
  "v_exp_f32 v152, v184\n\t" \
  "v_exp_f32 v153, v185\n\t" \
  "v_exp_f32 v154, v186\n\t" \
  "v_exp_f32 v155, v187\n\t" \
  "s_waitcnt lgkmcnt(1)\n\t" \
  "v_smfmac_f32_32x32x32_bf16 v[112:127], v[64:67], v[144:151], v52\n\t" \
  "v_exp_f32 v156, v188\n\t" \
  "v_exp_f32 v157, v189\n\t" \
  "v_smfmac_f32_16x16x64_bf16 v[72:75], v[48:51], v[144:151], v52\n\t" \
  "buffer_load_dwordx4 v55, s[84:87], s88 offen sc1 lds\n\t" \
  "s_add_u32 s88, s88, s89\n\t" \
  "s_add_u32 m0, m0, 0x400\n\t" \
  "v_exp_f32 v158, v190\n\t" \
  "v_exp_f32 v159, v191\n\t" \
  "v_mfma_f32_32x32x16_bf16 v[192:207], v[56:59], v[40:43], v[96:111]\n\t" \
  "ds_read_b128 v[56:59], %[an] offset:0\n\t" \
  "v_exp_f32 v144, v208\n\t" \
  "v_exp_f32 v145, v209\n\t" \
  "v_exp_f32 v146, v210\n\t" \
  "v_exp_f32 v147, v211\n\t" \
  "s_waitcnt lgkmcnt(1)\n\t" \
  "v_smfmac_f32_32x32x32_bf16 v[112:127], v[68:71], v[152:159], v52\n\t" \
  "v_exp_f32 v148, v212\n\t" \
  "v_exp_f32 v149, v213\n\t" \
  "v_smfmac_f32_16x16x64_bf16 v[72:75], v[48:51], v[152:159], v52\n\t" \
  "buffer_load_dwordx4 v55, s[84:87], s88 offen sc1 lds\n\t" \
  "s_add_u32 s88, s88, s89\n\t" \
  "s_add_u32 m0, m0, 0x400\n\t" \
  "v_exp_f32 v150, v214\n\t" \
  "v_exp_f32 v151, v215\n\t" \
  "v_mfma_f32_32x32x16_bf16 v[192:207], v[60:63], v[44:47], v[192:207]\n\t" \
  "ds_read_b128 v[60:63], %[an] offset:1024\n\t" \
  "v_exp_f32 v152, v216\n\t" \
  "v_exp_f32 v153, v217\n\t" \
  "v_exp_f32 v154, v218\n\t" \
  "v_exp_f32 v155, v219\n\t" \
  "v_smfmac_f32_32x32x32_bf16 v[128:143], v[64:67], v[144:151], v52\n\t" \
  "ds_read_b128 v[64:67], %[an] offset:2048\n\t" \
  "v_exp_f32 v156, v220\n\t" \
  "v_exp_f32 v157, v221\n\t" \
  "v_smfmac_f32_16x16x64_bf16 v[76:79], v[48:51], v[144:151], v52\n\t" \
  "buffer_load_dwordx4 v55, s[84:87], s88 offen sc1 lds\n\t" \
  "v_exp_f32 v158, v222\n\t" \
  "v_exp_f32 v159, v223\n\t" \
  "s_add_u32 s88, s88, 0x3400\n\t" \
  "s_swappc_b64 s[92:93], s[90:91]\n\t" \
  "v_mov_b32 %[ac], %[an]\n\t" \
  "s_mov_b32 %[pv], %[cs]\n\t" \
  "s_mov_b32 %[cs], %[nx]\n\t" \
  "s_sub_u32 %[c], %[c], 1\n\t" \
  "s_cmp_lg_u32 %[c], 0\n\t" \
  "s_cbranch_scc1 1b\n\t" \
  "s_setprio 0\n\t" \
  "s_waitcnt vmcnt(0) lgkmcnt(0)\n\t" \
  "v_smfmac_f32_32x32x32_bf16 v[128:143], v[68:71], v[152:159], v52\n\t" \
  "v_smfmac_f32_16x16x64_bf16 v[76:79], v[48:51], v[152:159], v52\n\t" \
  "s_nop 15\n\t" \
  ""
#define K3_ASM_RING_SLOW \
  "s_mov_b64 s[84:85], %[srdK0]\n\t" \
  "s_cmp_gt_u32 %[c], 5\n\t" \
  "s_cselect_b32 s86, 0, 0x40000\n\t" \
  "s_mov_b32 s87, 0x20000\n\t" \
  "s_mov_b32 s88, %[so]\n\t" \
  "s_movk_i32 s89, 0x400\n\t" \
  "v_mov_b32 v55, %[vlin]\n\t" \
  "s_add_u32 %[so], %[so], 0x4000\n\t" \
  "s_add_u32 m0, %[pv], %[dk]\n\t" \
  "s_cmp_lg_u32 %[c], 5\n\t" \
  "s_cbranch_scc1 6f\n\t" \
  "s_mov_b64 s[84:85], %[srdU0]\n\t" \
  "s_mov_b32 s86, %[lenU]\n\t" \
  "s_mov_b32 s88, %[sU]\n\t" \
  "s_mov_b32 s89, %[st8]\n\t" \
  "v_mov_b32 v55, v0\n\t" \
  "6:\n\t" \
  "s_cmp_lg_u32 %[c], 0\n\t" \
  "s_cbranch_scc1 30f\n\t" \
  "s_waitcnt vmcnt(4)\n\t" \
  "s_branch 4f\n\t" \
  "30:\n\t" \
  "s_cmp_lg_u32 %[c], 1\n\t" \
  "s_cbranch_scc1 31f\n\t" \
  "s_waitcnt vmcnt(0)\n\t" \
  "s_branch 4f\n\t" \
  "31:\n\t" \
  "s_cmp_lg_u32 %[c], 3\n\t" \
  "s_cbranch_scc1 33f\n\t" \
  "s_waitcnt vmcnt(4)\n\t" \
  "s_branch 4f\n\t" \
  "33:\n\t" \
  "s_cmp_lg_u32 %[c], 4\n\t" \
  "s_cbranch_scc1 34f\n\t" \
  "s_waitcnt vmcnt(8)\n\t" \
  "s_branch 4f\n\t" \
  "34:\n\t" \
  "s_cmp_lg_u32 %[c], 5\n\t" \
  "s_cbranch_scc1 35f\n\t" \
  "s_waitcnt vmcnt(0)\n\t" \
  "s_branch 4f\n\t" \
  "35:\n\t" \
  "4:\n\t" \
  "s_barrier\n\t" \
  "s_cmp_lg_u32 %[c], 0\n\t" \
  "s_cbranch_scc1 20f\n\t" \
  "s_mov_b32 %[nx2], m0\n\t" \
  "s_mov_b32 m0, %[dk]\n\t" \
  "s_nop 0\n\t" \
  "buffer_load_dwordx4 %[vlin], %[srdK], %[so2] offen offset:0 sc1 lds\n\t" \
  "buffer_load_dwordx4 %[vlin], %[srdK], %[so2] offen offset:1024 sc1 lds\n\t" \
  "buffer_load_dwordx4 %[vlin], %[srdK], %[so2] offen offset:2048 sc1 lds\n\t" \
  "buffer_load_dwordx4 %[vlin], %[srdK], %[so2] offen offset:3072 sc1 lds\n\t" \
  "s_mov_b32 m0, %[nx2]\n\t" \
  "s_branch 9f\n\t" \
  "20:\n\t" \
  "s_cmp_lg_u32 %[c], 1\n\t" \
  "s_cbranch_scc1 21f\n\t" \
  "v_xor_b32 v224, 0, v1\n\t" \
  "v_xor_b32 v225, 32, v1\n\t" \
  "v_xor_b32 v226, 64, v1\n\t" \
  "v_xor_b32 v227, 96, v1\n\t" \
  "ds_read_b64_tr_b16 v[240:241], v224\n\t" \
  "ds_read_b64_tr_b16 v[242:243], v224 offset:2048\n\t" \
  "ds_read_b64_tr_b16 v[244:245], v225\n\t" \
  "ds_read_b64_tr_b16 v[246:247], v225 offset:2048\n\t" \
  "ds_read_b64_tr_b16 v[248:249], v226\n\t" \
  "ds_read_b64_tr_b16 v[250:251], v226 offset:2048\n\t" \
  "ds_read_b64_tr_b16 v[252:253], v227\n\t" \
  "ds_read_b64_tr_b16 v[254:255], v227 offset:2048\n\t" \
  "s_waitcnt lgkmcnt(0)\n\t" \
  "v_mfma_f32_16x16x32_bf16 v[224:227], v[240:243], v[4:7], 0\n\t" \
  "v_mfma_f32_16x16x32_bf16 v[228:231], v[244:247], v[4:7], 0\n\t" \
  "v_mfma_f32_16x16x32_bf16 v[232:235], v[248:251], v[4:7], 0\n\t" \
  "v_mfma_f32_16x16x32_bf16 v[236:239], v[252:255], v[4:7], 0\n\t" \
  "v_lshrrev_b32 v240, 8, %[vlin]\n\t" \
  "v_lshlrev_b32 v240, 4, v240\n\t" \
  "v_add_u32 v240, %[dk], v240\n\t" \
  "v_add_u32 v240, 0x20000, v240\n\t" \
  "s_mov_b32 exec_lo, 0x00010001\n\t" \
  "s_mov_b32 exec_hi, 0x00010001\n\t" \
  "s_nop 5\n\t" \
  "ds_write_b128 v240, v[224:227] offset:0\n\t" \
  "ds_write_b128 v240, v[228:231] offset:64\n\t" \
  "ds_write_b128 v240, v[232:235] offset:128\n\t" \
  "ds_write_b128 v240, v[236:239] offset:192\n\t" \
  "s_mov_b64 exec, -1\n\t" \
  "s_branch 9f\n\t" \
  "21:\n\t" \
  "s_cmp_lg_u32 %[c], 2\n\t" \
  "s_cbranch_scc1 22f\n\t" \
  "s_cmp_lg_u32 %[wv], 0\n\t" \
  "s_cbranch_scc1 7f\n\t" \
  "v_lshrrev_b32 v240, 2, %[vlin]\n\t" \
  "v_add_u32 v241, %[dk], v240\n\t" \
  "v_add_u32 v241, 0x20000, v241\n\t" \
  "ds_read_b32 v224, v241 offset:0\n\t" \
  "ds_read_b32 v225, v241 offset:4096\n\t" \
  "ds_read_b32 v226, v241 offset:8192\n\t" \
  "ds_read_b32 v227, v241 offset:12288\n\t" \
  "ds_read_b32 v228, v241 offset:16384\n\t" \
  "ds_read_b32 v229, v241 offset:20480\n\t" \
  "ds_read_b32 v230, v241 offset:24576\n\t" \
  "ds_read_b32 v231, v241 offset:28672\n\t" \
  "s_waitcnt lgkmcnt(0)\n\t" \
  "v_add_f32 v224, v224, v225\n\t" \
  "v_add_f32 v226, v226, v227\n\t" \
  "v_add_f32 v228, v228, v229\n\t" \
  "v_add_f32 v230, v230, v231\n\t" \
  "v_add_f32 v224, v224, v226\n\t" \
  "v_add_f32 v228, v228, v230\n\t" \
  "v_add_f32 v224, v224, v228\n\t" \
  "buffer_store_dword v224, v240, %[srdX], %[oF] offen sc1\n\t" \
  "7:\n\t" \
  "s_branch 9f\n\t" \
  "22:\n\t" \
  "s_cmp_lg_u32 %[c], 3\n\t" \
  "s_cbranch_scc1 23f\n\t" \
  "s_cmp_lg_u32 %[wv], 0\n\t" \
  "s_cbranch_scc1 7f\n\t" \
  "v_mov_b32 v232, %[tl]\n\t" \
  "v_mov_b32 v233, %[th]\n\t" \
  "buffer_store_dwordx2 v[232:233], off, %[srdX], %[o3] sc1\n\t" \
  "7:\n\t" \
  "s_mov_b32 %[nx2], m0\n\t" \
  "s_add_u32 m0, %[dk], 0x20000\n\t" \
  "s_nop 0\n\t" \
  "buffer_load_dwordx4 v0, %[srdU], %[sU] offen lds\n\t" \
  "s_add_u32 %[sU], %[sU], %[st8]\n\t" \
  "s_add_u32 m0, m0, 0x400\n\t" \
  "s_nop 0\n\t" \
  "buffer_load_dwordx4 v0, %[srdU], %[sU] offen lds\n\t" \
  "s_add_u32 %[sU], %[sU], %[st8]\n\t" \
  "s_add_u32 m0, m0, 0x400\n\t" \
  "s_nop 0\n\t" \
  "buffer_load_dwordx4 v0, %[srdU], %[sU] offen lds\n\t" \
  "s_add_u32 %[sU], %[sU], %[st8]\n\t" \
  "s_add_u32 m0, m0, 0x400\n\t" \
  "s_nop 0\n\t" \
  "buffer_load_dwordx4 v0, %[srdU], %[sU] offen lds\n\t" \
  "s_add_u32 %[sU], %[sU], %[st8]\n\t" \
  "s_mov_b32 m0, %[nx2]\n\t" \
  "s_add_u32 %[sU], %[sU], %[sU2]\n\t" \
  "s_branch 9f\n\t" \
  "23:\n\t" \
  "s_cmp_lg_u32 %[c], 4\n\t" \
  "s_cbranch_scc1 24f\n\t" \
  "v_lshrrev_b32 v252, 1, %[vlin]\n\t" \
  "v_and_b32 v252, 0xff, v252\n\t" \
  "buffer_load_dwordx2 v[248:249], v252, %[srdX], %[of3] offen sc1\n\t" \
  "s_waitcnt vmcnt(0)\n\t" \
  "s_branch 9f\n\t" \
  "24:\n\t" \
  "s_cmp_lg_u32 %[c], 5\n\t" \
  "s_cbranch_scc1 25f\n\t" \
  "v_lshrrev_b32 v252, 1, %[vlin]\n\t" \
  "v_and_b32 v252, 0xff, v252\n\t" \
  "v_mov_b32 v250, %[tl]\n\t" \
  "v_mov_b32 v251, %[th]\n\t" \
  "s_mov_b32 %[nx2], 0x8000\n\t" \
  "8:\n\t" \
  "v_cmp_ne_u64 vcc, v[248:249], v[250:251]\n\t" \
  "s_nop 4\n\t" \
  "s_cbranch_vccz 7f\n\t" \
  "s_sub_u32 %[nx2], %[nx2], 1\n\t" \
  "s_cbranch_scc1 7f\n\t" \
  "s_sleep 1\n\t" \
  "buffer_load_dwordx2 v[248:249], v252, %[srdX], %[of3] offen sc1\n\t" \
  "s_waitcnt vmcnt(0)\n\t" \
  "s_branch 8b\n\t" \
  "7:\n\t" \
  "s_branch 9f\n\t" \
  "25:\n\t" \
  "s_cmp_lg_u32 %[c], 6\n\t" \
  "s_cbranch_scc1 26f\n\t" \
  "buffer_load_dwordx4 v[224:227], v2, %[srdX], 0 offen sc1\n\t" \
  "buffer_load_dwordx4 v[228:231], v2, %[srdX], 0 offen offset:64 sc1\n\t" \
  "buffer_load_dwordx4 v[232:235], v3, %[srdX], 0 offen sc1\n\t" \
  "s_mov_b32 %[nx2], 0x2000\n\t" \
  "buffer_load_dwordx4 v[236:239], v3, %[srdX], %[nx2] offen sc1\n\t" \
  "s_waitcnt vmcnt(0)\n\t" \
  "s_branch 9f\n\t" \
  "26:\n\t" \
  "9:\n\t" \
  "s_nop 0\n\t" \
  "buffer_load_dwordx4 v55, s[84:87], s88 offen sc1 lds\n\t" \
  "s_add_u32 s88, s88, s89\n\t" \
  "s_add_u32 m0, m0, 0x400\n\t" \
  "s_nop 0\n\t" \
  "buffer_load_dwordx4 v55, s[84:87], s88 offen sc1 lds\n\t" \
  "s_add_u32 s88, s88, s89\n\t" \
  "s_add_u32 m0, m0, 0x400\n\t" \
  "s_nop 0\n\t" \
  "buffer_load_dwordx4 v55, s[84:87], s88 offen sc1 lds\n\t" \
  "s_add_u32 s88, s88, s89\n\t" \
  "s_add_u32 m0, m0, 0x400\n\t" \
  "s_nop 0\n\t" \
  "buffer_load_dwordx4 v55, s[84:87], s88 offen sc1 lds\n\t" \
  "s_add_u32 s88, s88, s89\n\t" \
  "s_add_u32 m0, m0, 0x400\n\t" \
  "s_nop 0\n\t" \
  ""
#define QP_ASM_A \
  "buffer_load_dwordx4 v[96:99], %[vlin], %[srdT], %[sT] offen offset:0 sc1\n\t" \
  "buffer_load_dwordx4 v[100:103], %[vlin], %[srdT], %[sT] offen offset:1024 sc1\n\t" \
  "buffer_load_dwordx4 v[104:107], %[vlin], %[srdT], %[sT] offen offset:2048 sc1\n\t" \
  "buffer_load_dwordx4 v[108:111], %[vlin], %[srdT], %[sT] offen offset:3072 sc1\n\t" \
  "s_add_u32 %[sT], %[sT], 0x1000\n\t" \
  "s_barrier\n\t" \
  "buffer_load_dwordx4 v[112:115], %[vlin], %[srdT], %[sT] offen offset:0 sc1\n\t" \
  "buffer_load_dwordx4 v[116:119], %[vlin], %[srdT], %[sT] offen offset:1024 sc1\n\t" \
  "buffer_load_dwordx4 v[120:123], %[vlin], %[srdT], %[sT] offen offset:2048 sc1\n\t" \
  "buffer_load_dwordx4 v[124:127], %[vlin], %[srdT], %[sT] offen offset:3072 sc1\n\t" \
  "s_add_u32 %[sT], %[sT], 0x1000\n\t" \
  "s_barrier\n\t" \
  "buffer_load_dwordx4 v[128:131], %[vlin], %[srdT], %[sT] offen offset:0 sc1\n\t" \
  "buffer_load_dwordx4 v[132:135], %[vlin], %[srdT], %[sT] offen offset:1024 sc1\n\t" \
  "buffer_load_dwordx4 v[136:139], %[vlin], %[srdT], %[sT] offen offset:2048 sc1\n\t" \
  "buffer_load_dwordx4 v[140:143], %[vlin], %[srdT], %[sT] offen offset:3072 sc1\n\t" \
  "s_add_u32 %[sT], %[sT], 0x1000\n\t" \
  "buffer_load_dwordx4 v[144:147], %[vlin], %[srdT], %[sT] offen offset:0 sc1\n\t" \
  "buffer_load_dwordx4 v[148:151], %[vlin], %[srdT], %[sT] offen offset:1024 sc1\n\t" \
  "buffer_load_dwordx4 v[152:155], %[vlin], %[srdT], %[sT] offen offset:2048 sc1\n\t" \
  "buffer_load_dwordx4 v[156:159], %[vlin], %[srdT], %[sT] offen offset:3072 sc1\n\t" \
  "s_add_u32 %[sT], %[sT], 0x1000\n\t" \
  "v_mov_b64 v[32:33], 0\n\t" \
  "v_mov_b64 v[34:35], 0\n\t" \
  "v_mov_b64 v[36:37], 0\n\t" \
  "v_mov_b64 v[38:39], 0\n\t" \
  "v_mov_b64 v[40:41], 0\n\t" \
  "v_mov_b64 v[42:43], 0\n\t" \
  "v_mov_b64 v[44:45], 0\n\t" \
  "v_mov_b64 v[46:47], 0\n\t" \
  "v_mov_b64 v[48:49], 0\n\t" \
  "v_mov_b64 v[50:51], 0\n\t" \
  "v_mov_b64 v[52:53], 0\n\t" \
  "v_mov_b64 v[54:55], 0\n\t" \
  "v_mov_b64 v[56:57], 0\n\t" \
  "v_mov_b64 v[58:59], 0\n\t" \
  "v_mov_b64 v[60:61], 0\n\t" \
  "v_mov_b64 v[62:63], 0\n\t" \
  "v_mov_b32 v64, %[tl]\n\t" \
  "v_mov_b32 v65, %[th]\n\t" \
  "s_waitcnt vmcnt(16)\n\t" \
  "s_cmp_lg_u32 %[ld], 0\n\t" \
  "s_cbranch_scc1 1f\n\t" \
  "buffer_store_dwordx2 v[64:65], off, %[srdX], %[oK] sc1\n\t" \
  "1:\n\t" \
  "s_barrier\n\t" \
  "ds_read_b64_tr_b16 v[64:65], %[a0] offset:0\n\t" \
  "ds_read_b64_tr_b16 v[66:67], %[a0] offset:1024\n\t" \
  "ds_read_b64_tr_b16 v[68:69], %[a1] offset:0\n\t" \
  "ds_read_b64_tr_b16 v[70:71], %[a1] offset:1024\n\t" \
  "ds_read_b64_tr_b16 v[72:73], %[a0] offset:2048\n\t" \
  "ds_read_b64_tr_b16 v[74:75], %[a0] offset:3072\n\t" \
  "ds_read_b64_tr_b16 v[76:77], %[a1] offset:2048\n\t" \
  "ds_read_b64_tr_b16 v[78:79], %[a1] offset:3072\n\t" \
  "ds_read_b64_tr_b16 v[80:81], %[a0] offset:4096\n\t" \
  "ds_read_b64_tr_b16 v[82:83], %[a0] offset:5120\n\t" \
  "ds_read_b64_tr_b16 v[84:85], %[a1] offset:4096\n\t" \
  "ds_read_b64_tr_b16 v[86:87], %[a1] offset:5120\n\t" \
  "ds_read_b64_tr_b16 v[88:89], %[a0] offset:6144\n\t" \
  "ds_read_b64_tr_b16 v[90:91], %[a0] offset:7168\n\t" \
  "ds_read_b64_tr_b16 v[92:93], %[a1] offset:6144\n\t" \
  "ds_read_b64_tr_b16 v[94:95], %[a1] offset:7168\n\t" \
  "s_waitcnt vmcnt(12)\n\t" \
  "s_waitcnt lgkmcnt(14)\n\t" \
  "v_mfma_f32_32x32x16_bf16 v[32:47], v[64:67], v[96:99], v[32:47]\n\t" \
  "ds_read_b64_tr_b16 v[64:65], %[a0] offset:16384\n\t" \
  "ds_read_b64_tr_b16 v[66:67], %[a0] offset:17408\n\t" \
  "s_waitcnt lgkmcnt(14)\n\t" \
  "v_mfma_f32_32x32x16_bf16 v[48:63], v[68:71], v[96:99], v[48:63]\n\t" \
  "ds_read_b64_tr_b16 v[68:69], %[a1] offset:16384\n\t" \
  "ds_read_b64_tr_b16 v[70:71], %[a1] offset:17408\n\t" \
  "buffer_load_dwordx4 v[96:99], %[vlin], %[srdT], %[sT] offen offset:0 sc1\n\t" \
  "s_waitcnt lgkmcnt(14)\n\t" \
  "v_mfma_f32_32x32x16_bf16 v[32:47], v[72:75], v[100:103], v[32:47]\n\t" \
  "ds_read_b64_tr_b16 v[72:73], %[a0] offset:18432\n\t" \
  "ds_read_b64_tr_b16 v[74:75], %[a0] offset:19456\n\t" \
  "s_waitcnt lgkmcnt(14)\n\t" \
  "v_mfma_f32_32x32x16_bf16 v[48:63], v[76:79], v[100:103], v[48:63]\n\t" \
  "ds_read_b64_tr_b16 v[76:77], %[a1] offset:18432\n\t" \
  "ds_read_b64_tr_b16 v[78:79], %[a1] offset:19456\n\t" \
  "buffer_load_dwordx4 v[100:103], %[vlin], %[srdT], %[sT] offen offset:1024 sc1\n\t" \
  "s_waitcnt lgkmcnt(14)\n\t" \
  "v_mfma_f32_32x32x16_bf16 v[32:47], v[80:83], v[104:107], v[32:47]\n\t" \
  "ds_read_b64_tr_b16 v[80:81], %[a0] offset:20480\n\t" \
  "ds_read_b64_tr_b16 v[82:83], %[a0] offset:21504\n\t" \
  "s_waitcnt lgkmcnt(14)\n\t" \
  "v_mfma_f32_32x32x16_bf16 v[48:63], v[84:87], v[104:107], v[48:63]\n\t" \
  "ds_read_b64_tr_b16 v[84:85], %[a1] offset:20480\n\t" \
  "ds_read_b64_tr_b16 v[86:87], %[a1] offset:21504\n\t" \
  "buffer_load_dwordx4 v[104:107], %[vlin], %[srdT], %[sT] offen offset:2048 sc1\n\t" \
  "s_waitcnt lgkmcnt(14)\n\t" \
  "v_mfma_f32_32x32x16_bf16 v[32:47], v[88:91], v[108:111], v[32:47]\n\t" \
  "ds_read_b64_tr_b16 v[88:89], %[a0] offset:22528\n\t" \
  "ds_read_b64_tr_b16 v[90:91], %[a0] offset:23552\n\t" \
  "s_waitcnt lgkmcnt(14)\n\t" \
  "v_mfma_f32_32x32x16_bf16 v[48:63], v[92:95], v[108:111], v[48:63]\n\t" \
  "ds_read_b64_tr_b16 v[92:93], %[a1] offset:22528\n\t" \
  "ds_read_b64_tr_b16 v[94:95], %[a1] offset:23552\n\t" \
  "buffer_load_dwordx4 v[108:111], %[vlin], %[srdT], %[sT] offen offset:3072 sc1\n\t" \
  "s_add_u32 %[sT], %[sT], 0x1000\n\t" \
  "s_waitcnt vmcnt(12)\n\t" \
  "s_barrier\n\t" \
  "s_waitcnt lgkmcnt(14)\n\t" \
  "v_mfma_f32_32x32x16_bf16 v[32:47], v[64:67], v[112:115], v[32:47]\n\t" \
  "s_add_u32 m0, %[dW], 0\n\t" \
  "ds_read_b64_tr_b16 v[64:65], %[a0] offset:32768\n\t" \
  "ds_read_b64_tr_b16 v[66:67], %[a0] offset:33792\n\t" \
  "buffer_load_dwordx4 %[vW], %[srdW], %[sW] offen lds\n\t" \
  "s_waitcnt lgkmcnt(14)\n\t" \
  "v_mfma_f32_32x32x16_bf16 v[48:63], v[68:71], v[112:115], v[48:63]\n\t" \
  "ds_read_b64_tr_b16 v[68:69], %[a1] offset:32768\n\t" \
  "ds_read_b64_tr_b16 v[70:71], %[a1] offset:33792\n\t" \
  "buffer_load_dwordx4 v[112:115], %[vlin], %[srdT], %[sT] offen offset:0 sc1\n\t" \
  "s_waitcnt lgkmcnt(14)\n\t" \
  "v_mfma_f32_32x32x16_bf16 v[32:47], v[72:75], v[116:119], v[32:47]\n\t" \
  "ds_read_b64_tr_b16 v[72:73], %[a0] offset:34816\n\t" \
  "ds_read_b64_tr_b16 v[74:75], %[a0] offset:35840\n\t" \
  "s_waitcnt lgkmcnt(14)\n\t" \
  "v_mfma_f32_32x32x16_bf16 v[48:63], v[76:79], v[116:119], v[48:63]\n\t" \
  "ds_read_b64_tr_b16 v[76:77], %[a1] offset:34816\n\t" \
  "ds_read_b64_tr_b16 v[78:79], %[a1] offset:35840\n\t" \
  "buffer_load_dwordx4 v[116:119], %[vlin], %[srdT], %[sT] offen offset:1024 sc1\n\t" \
  "s_waitcnt lgkmcnt(14)\n\t" \
  "v_mfma_f32_32x32x16_bf16 v[32:47], v[80:83], v[120:123], v[32:47]\n\t" \
  "s_add_u32 m0, %[dW], 1024\n\t" \
  "ds_read_b64_tr_b16 v[80:81], %[a0] offset:36864\n\t" \
  "ds_read_b64_tr_b16 v[82:83], %[a0] offset:37888\n\t" \
  "buffer_load_dwordx4 %[vW2], %[srdW], %[sW] offen lds\n\t" \
  "s_add_u32 %[sW], %[sW], 131072\n\t" \
  "s_waitcnt lgkmcnt(14)\n\t" \
  "v_mfma_f32_32x32x16_bf16 v[48:63], v[84:87], v[120:123], v[48:63]\n\t" \
  "ds_read_b64_tr_b16 v[84:85], %[a1] offset:36864\n\t" \
  "ds_read_b64_tr_b16 v[86:87], %[a1] offset:37888\n\t" \
  "buffer_load_dwordx4 v[120:123], %[vlin], %[srdT], %[sT] offen offset:2048 sc1\n\t" \
  "s_waitcnt lgkmcnt(14)\n\t" \
  "v_mfma_f32_32x32x16_bf16 v[32:47], v[88:91], v[124:127], v[32:47]\n\t" \
  "ds_read_b64_tr_b16 v[88:89], %[a0] offset:38912\n\t" \
  "ds_read_b64_tr_b16 v[90:91], %[a0] offset:39936\n\t" \
  "s_waitcnt lgkmcnt(14)\n\t" \
  "v_mfma_f32_32x32x16_bf16 v[48:63], v[92:95], v[124:127], v[48:63]\n\t" \
  "ds_read_b64_tr_b16 v[92:93], %[a1] offset:38912\n\t" \
  "ds_read_b64_tr_b16 v[94:95], %[a1] offset:39936\n\t" \
  "buffer_load_dwordx4 v[124:127], %[vlin], %[srdT], %[sT] offen offset:3072 sc1\n\t" \
  "s_add_u32 %[sT], %[sT], 0x1000\n\t" \
  "s_waitcnt vmcnt(14)\n\t" \
  "s_barrier\n\t" \
  "s_waitcnt lgkmcnt(14)\n\t" \
  "v_mfma_f32_32x32x16_bf16 v[32:47], v[64:67], v[128:131], v[32:47]\n\t" \
  "s_add_u32 m0, %[dW], 16384\n\t" \
  "ds_read_b64_tr_b16 v[64:65], %[a0] offset:49152\n\t" \
  "ds_read_b64_tr_b16 v[66:67], %[a0] offset:50176\n\t" \
  "buffer_load_dwordx4 %[vW], %[srdW], %[sW] offen lds\n\t" \
  "s_waitcnt lgkmcnt(14)\n\t" \
  "v_mfma_f32_32x32x16_bf16 v[48:63], v[68:71], v[128:131], v[48:63]\n\t" \
  "ds_read_b64_tr_b16 v[68:69], %[a1] offset:49152\n\t" \
  "ds_read_b64_tr_b16 v[70:71], %[a1] offset:50176\n\t" \
  "buffer_load_dwordx4 v[128:131], %[vlin], %[srdT], %[sT] offen offset:0 sc1\n\t" \
  "s_waitcnt lgkmcnt(14)\n\t" \
  "v_mfma_f32_32x32x16_bf16 v[32:47], v[72:75], v[132:135], v[32:47]\n\t" \
  "ds_read_b64_tr_b16 v[72:73], %[a0] offset:51200\n\t" \
  "ds_read_b64_tr_b16 v[74:75], %[a0] offset:52224\n\t" \
  "s_waitcnt lgkmcnt(14)\n\t" \
  "v_mfma_f32_32x32x16_bf16 v[48:63], v[76:79], v[132:135], v[48:63]\n\t" \
  "ds_read_b64_tr_b16 v[76:77], %[a1] offset:51200\n\t" \
  "ds_read_b64_tr_b16 v[78:79], %[a1] offset:52224\n\t" \
  "buffer_load_dwordx4 v[132:135], %[vlin], %[srdT], %[sT] offen offset:1024 sc1\n\t" \
  "s_waitcnt lgkmcnt(14)\n\t" \
  "v_mfma_f32_32x32x16_bf16 v[32:47], v[80:83], v[136:139], v[32:47]\n\t" \
  "s_add_u32 m0, %[dW], 17408\n\t" \
  "ds_read_b64_tr_b16 v[80:81], %[a0] offset:53248\n\t" \
  "ds_read_b64_tr_b16 v[82:83], %[a0] offset:54272\n\t" \
  "buffer_load_dwordx4 %[vW2], %[srdW], %[sW] offen lds\n\t" \
  "s_add_u32 %[sW], %[sW], 131072\n\t" \
  "s_waitcnt lgkmcnt(14)\n\t" \
  "v_mfma_f32_32x32x16_bf16 v[48:63], v[84:87], v[136:139], v[48:63]\n\t" \
  "ds_read_b64_tr_b16 v[84:85], %[a1] offset:53248\n\t" \
  "ds_read_b64_tr_b16 v[86:87], %[a1] offset:54272\n\t" \
  "buffer_load_dwordx4 v[136:139], %[vlin], %[srdT], %[sT] offen offset:2048 sc1\n\t" \
  "s_waitcnt lgkmcnt(14)\n\t" \
  "v_mfma_f32_32x32x16_bf16 v[32:47], v[88:91], v[140:143], v[32:47]\n\t" \
  "ds_read_b64_tr_b16 v[88:89], %[a0] offset:55296\n\t" \
  "ds_read_b64_tr_b16 v[90:91], %[a0] offset:56320\n\t" \
  "s_waitcnt lgkmcnt(14)\n\t" \
  "v_mfma_f32_32x32x16_bf16 v[48:63], v[92:95], v[140:143], v[48:63]\n\t" \
  "ds_read_b64_tr_b16 v[92:93], %[a1] offset:55296\n\t" \
  "ds_read_b64_tr_b16 v[94:95], %[a1] offset:56320\n\t" \
  "buffer_load_dwordx4 v[140:143], %[vlin], %[srdT], %[sT] offen offset:3072 sc1\n\t" \
  "s_add_u32 %[sT], %[sT], 0x1000\n\t" \
  "s_waitcnt vmcnt(16)\n\t" \
  "s_waitcnt lgkmcnt(14)\n\t" \
  "v_mfma_f32_32x32x16_bf16 v[32:47], v[64:67], v[144:147], v[32:47]\n\t" \
  "ds_read_b64_tr_b16 v[64:65], %[b0] offset:0\n\t" \
  "ds_read_b64_tr_b16 v[66:67], %[b0] offset:1024\n\t" \
  "s_waitcnt lgkmcnt(14)\n\t" \
  "v_mfma_f32_32x32x16_bf16 v[48:63], v[68:71], v[144:147], v[48:63]\n\t" \
  "ds_read_b64_tr_b16 v[68:69], %[b1] offset:0\n\t" \
  "ds_read_b64_tr_b16 v[70:71], %[b1] offset:1024\n\t" \
  "buffer_load_dwordx4 v[144:147], %[vlin], %[srdT], %[sT] offen offset:0 sc1\n\t" \
  "s_waitcnt lgkmcnt(14)\n\t" \
  "v_mfma_f32_32x32x16_bf16 v[32:47], v[72:75], v[148:151], v[32:47]\n\t" \
  "ds_read_b64_tr_b16 v[72:73], %[b0] offset:2048\n\t" \
  "ds_read_b64_tr_b16 v[74:75], %[b0] offset:3072\n\t" \
  "s_waitcnt lgkmcnt(14)\n\t" \
  "v_mfma_f32_32x32x16_bf16 v[48:63], v[76:79], v[148:151], v[48:63]\n\t" \
  "ds_read_b64_tr_b16 v[76:77], %[b1] offset:2048\n\t" \
  "ds_read_b64_tr_b16 v[78:79], %[b1] offset:3072\n\t" \
  "buffer_load_dwordx4 v[148:151], %[vlin], %[srdT], %[sT] offen offset:1024 sc1\n\t" \
  "s_waitcnt lgkmcnt(14)\n\t" \
  "v_mfma_f32_32x32x16_bf16 v[32:47], v[80:83], v[152:155], v[32:47]\n\t" \
  "ds_read_b64_tr_b16 v[80:81], %[b0] offset:4096\n\t" \
  "ds_read_b64_tr_b16 v[82:83], %[b0] offset:5120\n\t" \
  "s_waitcnt lgkmcnt(14)\n\t" \
  "v_mfma_f32_32x32x16_bf16 v[48:63], v[84:87], v[152:155], v[48:63]\n\t" \
  "ds_read_b64_tr_b16 v[84:85], %[b1] offset:4096\n\t" \
  "ds_read_b64_tr_b16 v[86:87], %[b1] offset:5120\n\t" \
  "buffer_load_dwordx4 v[152:155], %[vlin], %[srdT], %[sT] offen offset:2048 sc1\n\t" \
  "s_waitcnt lgkmcnt(14)\n\t" \
  "v_mfma_f32_32x32x16_bf16 v[32:47], v[88:91], v[156:159], v[32:47]\n\t" \
  "ds_read_b64_tr_b16 v[88:89], %[b0] offset:6144\n\t" \
  "ds_read_b64_tr_b16 v[90:91], %[b0] offset:7168\n\t" \
  "s_waitcnt lgkmcnt(14)\n\t" \
  "v_mfma_f32_32x32x16_bf16 v[48:63], v[92:95], v[156:159], v[48:63]\n\t" \
  "ds_read_b64_tr_b16 v[92:93], %[b1] offset:6144\n\t" \
  "ds_read_b64_tr_b16 v[94:95], %[b1] offset:7168\n\t" \
  "buffer_load_dwordx4 v[156:159], %[vlin], %[srdT], %[sT] offen offset:3072 sc1\n\t" \
  "s_add_u32 %[sT], %[sT], 0x1000\n\t" \
  "s_waitcnt vmcnt(16)\n\t" \
  "buffer_load_dwordx2 %[fl], %[vfl], %[srdX], 0 offen sc1\n\t" \
  "s_waitcnt lgkmcnt(14)\n\t" \
  "v_mfma_f32_32x32x16_bf16 v[32:47], v[64:67], v[96:99], v[32:47]\n\t" \
  "ds_read_b64_tr_b16 v[64:65], %[b0] offset:16384\n\t" \
  "ds_read_b64_tr_b16 v[66:67], %[b0] offset:17408\n\t" \
  "s_waitcnt lgkmcnt(14)\n\t" \
  "v_mfma_f32_32x32x16_bf16 v[48:63], v[68:71], v[96:99], v[48:63]\n\t" \
  "ds_read_b64_tr_b16 v[68:69], %[b1] offset:16384\n\t" \
  "ds_read_b64_tr_b16 v[70:71], %[b1] offset:17408\n\t" \
  "s_waitcnt lgkmcnt(14)\n\t" \
  "v_mfma_f32_32x32x16_bf16 v[32:47], v[72:75], v[100:103], v[32:47]\n\t" \
  "ds_read_b64_tr_b16 v[72:73], %[b0] offset:18432\n\t" \
  "ds_read_b64_tr_b16 v[74:75], %[b0] offset:19456\n\t" \
  "s_waitcnt lgkmcnt(14)\n\t" \
  "v_mfma_f32_32x32x16_bf16 v[48:63], v[76:79], v[100:103], v[48:63]\n\t" \
  "ds_read_b64_tr_b16 v[76:77], %[b1] offset:18432\n\t" \
  "ds_read_b64_tr_b16 v[78:79], %[b1] offset:19456\n\t" \
  "s_waitcnt lgkmcnt(14)\n\t" \
  "v_mfma_f32_32x32x16_bf16 v[32:47], v[80:83], v[104:107], v[32:47]\n\t" \
  "ds_read_b64_tr_b16 v[80:81], %[b0] offset:20480\n\t" \
  "ds_read_b64_tr_b16 v[82:83], %[b0] offset:21504\n\t" \
  "s_waitcnt lgkmcnt(14)\n\t" \
  "v_mfma_f32_32x32x16_bf16 v[48:63], v[84:87], v[104:107], v[48:63]\n\t" \
  "ds_read_b64_tr_b16 v[84:85], %[b1] offset:20480\n\t" \
  "ds_read_b64_tr_b16 v[86:87], %[b1] offset:21504\n\t" \
  "s_waitcnt lgkmcnt(14)\n\t" \
  "v_mfma_f32_32x32x16_bf16 v[32:47], v[88:91], v[108:111], v[32:47]\n\t" \
  "ds_read_b64_tr_b16 v[88:89], %[b0] offset:22528\n\t" \
  "ds_read_b64_tr_b16 v[90:91], %[b0] offset:23552\n\t" \
  "s_waitcnt lgkmcnt(14)\n\t" \
  "v_mfma_f32_32x32x16_bf16 v[48:63], v[92:95], v[108:111], v[48:63]\n\t" \
  "ds_read_b64_tr_b16 v[92:93], %[b1] offset:22528\n\t" \
  "ds_read_b64_tr_b16 v[94:95], %[b1] offset:23552\n\t" \
  "s_waitcnt vmcnt(0)\n\t" \
  ""
#define QP_ASM_B \
  "s_barrier\n\t" \
  "s_waitcnt lgkmcnt(14)\n\t" \
  "v_mfma_f32_32x32x16_bf16 v[32:47], v[64:67], v[112:115], v[32:47]\n\t" \
  "ds_read_b64_tr_b16 v[64:65], %[a0] offset:0\n\t" \
  "ds_read_b64_tr_b16 v[66:67], %[a0] offset:1024\n\t" \
  "buffer_load_dwordx4 %[k0], %[vlin], %[srdX], %[sK0] offen sc1\n\t" \
  "s_waitcnt lgkmcnt(14)\n\t" \
  "v_mfma_f32_32x32x16_bf16 v[48:63], v[68:71], v[112:115], v[48:63]\n\t" \
  "ds_read_b64_tr_b16 v[68:69], %[a1] offset:0\n\t" \
  "ds_read_b64_tr_b16 v[70:71], %[a1] offset:1024\n\t" \
  "buffer_load_dwordx4 %[k1], %[vlin], %[srdX], %[sK0] offen offset:1024 sc1\n\t" \
  "s_waitcnt lgkmcnt(14)\n\t" \
  "v_mfma_f32_32x32x16_bf16 v[32:47], v[72:75], v[116:119], v[32:47]\n\t" \
  "ds_read_b64_tr_b16 v[72:73], %[a0] offset:2048\n\t" \
  "ds_read_b64_tr_b16 v[74:75], %[a0] offset:3072\n\t" \
  "buffer_load_dwordx4 %[kst], %[vks], %[srdX], 0 offen sc1\n\t" \
  "s_waitcnt lgkmcnt(14)\n\t" \
  "v_mfma_f32_32x32x16_bf16 v[48:63], v[76:79], v[116:119], v[48:63]\n\t" \
  "ds_read_b64_tr_b16 v[76:77], %[a1] offset:2048\n\t" \
  "ds_read_b64_tr_b16 v[78:79], %[a1] offset:3072\n\t" \
  "s_add_u32 m0, %[dk], 65536\n\t" \
  "s_nop 0\n\t" \
  "buffer_load_dwordx4 %[vlin], %[srdK], %[so] offen offset:0 sc1 lds\n\t" \
  "s_waitcnt lgkmcnt(14)\n\t" \
  "v_mfma_f32_32x32x16_bf16 v[32:47], v[80:83], v[120:123], v[32:47]\n\t" \
  "ds_read_b64_tr_b16 v[80:81], %[a0] offset:4096\n\t" \
  "ds_read_b64_tr_b16 v[82:83], %[a0] offset:5120\n\t" \
  "buffer_load_dwordx4 %[vlin], %[srdK], %[so] offen offset:1024 sc1 lds\n\t" \
  "s_waitcnt lgkmcnt(14)\n\t" \
  "v_mfma_f32_32x32x16_bf16 v[48:63], v[84:87], v[120:123], v[48:63]\n\t" \
  "ds_read_b64_tr_b16 v[84:85], %[a1] offset:4096\n\t" \
  "ds_read_b64_tr_b16 v[86:87], %[a1] offset:5120\n\t" \
  "buffer_load_dwordx4 %[vlin], %[srdK], %[so] offen offset:2048 sc1 lds\n\t" \
  "s_waitcnt lgkmcnt(14)\n\t" \
  "v_mfma_f32_32x32x16_bf16 v[32:47], v[88:91], v[124:127], v[32:47]\n\t" \
  "ds_read_b64_tr_b16 v[88:89], %[a0] offset:6144\n\t" \
  "ds_read_b64_tr_b16 v[90:91], %[a0] offset:7168\n\t" \
  "buffer_load_dwordx4 %[vlin], %[srdK], %[so] offen offset:3072 sc1 lds\n\t" \
  "s_waitcnt lgkmcnt(14)\n\t" \
  "v_mfma_f32_32x32x16_bf16 v[48:63], v[92:95], v[124:127], v[48:63]\n\t" \
  "ds_read_b64_tr_b16 v[92:93], %[a1] offset:6144\n\t" \
  "ds_read_b64_tr_b16 v[94:95], %[a1] offset:7168\n\t" \
  "s_add_u32 %[so], %[so], 0x4000\n\t" \
  "s_waitcnt lgkmcnt(14)\n\t" \
  "v_mfma_f32_32x32x16_bf16 v[32:47], v[64:67], v[128:131], v[32:47]\n\t" \
  "ds_read_b64_tr_b16 v[64:65], %[a0] offset:16384\n\t" \
  "ds_read_b64_tr_b16 v[66:67], %[a0] offset:17408\n\t" \
  "s_waitcnt lgkmcnt(14)\n\t" \
  "v_mfma_f32_32x32x16_bf16 v[48:63], v[68:71], v[128:131], v[48:63]\n\t" \
  "ds_read_b64_tr_b16 v[68:69], %[a1] offset:16384\n\t" \
  "ds_read_b64_tr_b16 v[70:71], %[a1] offset:17408\n\t" \
  "s_waitcnt lgkmcnt(14)\n\t" \
  "v_mfma_f32_32x32x16_bf16 v[32:47], v[72:75], v[132:135], v[32:47]\n\t" \
  "ds_read_b64_tr_b16 v[72:73], %[a0] offset:18432\n\t" \
  "ds_read_b64_tr_b16 v[74:75], %[a0] offset:19456\n\t" \
  "s_waitcnt lgkmcnt(14)\n\t" \
  "v_mfma_f32_32x32x16_bf16 v[48:63], v[76:79], v[132:135], v[48:63]\n\t" \
  "ds_read_b64_tr_b16 v[76:77], %[a1] offset:18432\n\t" \
  "ds_read_b64_tr_b16 v[78:79], %[a1] offset:19456\n\t" \
  "s_waitcnt lgkmcnt(14)\n\t" \
  "v_mfma_f32_32x32x16_bf16 v[32:47], v[80:83], v[136:139], v[32:47]\n\t" \
  "ds_read_b64_tr_b16 v[80:81], %[a0] offset:20480\n\t" \
  "ds_read_b64_tr_b16 v[82:83], %[a0] offset:21504\n\t" \
  "s_waitcnt lgkmcnt(14)\n\t" \
  "v_mfma_f32_32x32x16_bf16 v[48:63], v[84:87], v[136:139], v[48:63]\n\t" \
  "ds_read_b64_tr_b16 v[84:85], %[a1] offset:20480\n\t" \
  "ds_read_b64_tr_b16 v[86:87], %[a1] offset:21504\n\t" \
  "s_waitcnt lgkmcnt(14)\n\t" \
  "v_mfma_f32_32x32x16_bf16 v[32:47], v[88:91], v[140:143], v[32:47]\n\t" \
  "ds_read_b64_tr_b16 v[88:89], %[a0] offset:22528\n\t" \
  "ds_read_b64_tr_b16 v[90:91], %[a0] offset:23552\n\t" \
  "s_waitcnt lgkmcnt(14)\n\t" \
  "v_mfma_f32_32x32x16_bf16 v[48:63], v[92:95], v[140:143], v[48:63]\n\t" \
  "ds_read_b64_tr_b16 v[92:93], %[a1] offset:22528\n\t" \
  "ds_read_b64_tr_b16 v[94:95], %[a1] offset:23552\n\t" \
  "s_waitcnt lgkmcnt(14)\n\t" \
  "v_mfma_f32_32x32x16_bf16 v[32:47], v[64:67], v[144:147], v[32:47]\n\t" \
  "s_waitcnt lgkmcnt(12)\n\t" \
  "v_mfma_f32_32x32x16_bf16 v[48:63], v[68:71], v[144:147], v[48:63]\n\t" \
  "s_waitcnt lgkmcnt(10)\n\t" \
  "v_mfma_f32_32x32x16_bf16 v[32:47], v[72:75], v[148:151], v[32:47]\n\t" \
  "s_waitcnt lgkmcnt(8)\n\t" \
  "v_mfma_f32_32x32x16_bf16 v[48:63], v[76:79], v[148:151], v[48:63]\n\t" \
  "s_waitcnt lgkmcnt(6)\n\t" \
  "v_mfma_f32_32x32x16_bf16 v[32:47], v[80:83], v[152:155], v[32:47]\n\t" \
  "s_waitcnt lgkmcnt(4)\n\t" \
  "v_mfma_f32_32x32x16_bf16 v[48:63], v[84:87], v[152:155], v[48:63]\n\t" \
  "s_waitcnt lgkmcnt(2)\n\t" \
  "v_mfma_f32_32x32x16_bf16 v[32:47], v[88:91], v[156:159], v[32:47]\n\t" \
  "s_waitcnt lgkmcnt(0)\n\t" \
  "v_mfma_f32_32x32x16_bf16 v[48:63], v[92:95], v[156:159], v[48:63]\n\t" \
  "s_waitcnt vmcnt(4)\n\t" \
  "s_barrier\n\t" \
  "s_nop 15\n\t" \
  ""
#define K5_ASM_LOOP \
  "v_add_u32 v118, %[d6], v0\n\t" \
  "v_add_u32 v242, %[e6], v241\n\t" \
  "ds_read_b32 v60, v240 offset:0\n\t" \
  "ds_read_b32 v61, v240 offset:64\n\t" \
  "ds_read_b32 v62, v240 offset:128\n\t" \
  "ds_read_b32 v63, v240 offset:192\n\t" \
  "ds_read_b128 v[32:35], v241 offset:0\n\t" \
  "ds_read_b128 v[36:39], v241 offset:32\n\t" \
  "ds_read_b128 v[40:43], v241 offset:64\n\t" \
  "ds_read_b128 v[44:47], v241 offset:96\n\t" \
  "ds_read_b128 v[48:51], v241 offset:128\n\t" \
  "ds_read_b128 v[52:55], v241 offset:160\n\t" \
  "ds_read_b128 v[56:59], v242\n\t" \
  "s_add_u32 m0, %[dT], 61472\n\t" \
  "s_sub_u32 %[cnt], %[sT], 2048\n\t" \
  "buffer_load_dwordx4 v2, %[srdY], %[cnt] offen sc1 lds\n\t" \
  "s_add_u32 m0, %[dT], 62496\n\t" \
  "s_nop 0\n\t" \
  "buffer_load_dwordx4 v3, %[srdY], %[cnt] offen sc1 lds\n\t" \
  "s_add_u32 m0, %[dT], 92208\n\t" \
  "s_sub_u32 %[cnt], %[sT], 1024\n\t" \
  "buffer_load_dwordx4 v2, %[srdY], %[cnt] offen sc1 lds\n\t" \
  "s_add_u32 m0, %[dT], 93232\n\t" \
  "s_nop 0\n\t" \
  "buffer_load_dwordx4 v3, %[srdY], %[cnt] offen sc1 lds\n\t" \
  "s_waitcnt lgkmcnt(0)\n\t" \
  "v_pk_mul_f32 v[128:129], v[32:33], v[60:61] op_sel_hi:[1,0]\n\t" \
  "v_pk_mul_f32 v[130:131], v[34:35], v[60:61] op_sel_hi:[1,0]\n\t" \
  "ds_read_b128 v[100:103], v1 offset:0\n\t" \
  "v_pk_mul_f32 v[132:133], v[32:33], v[60:61] op_sel:[0,1]\n\t" \
  "v_pk_mul_f32 v[134:135], v[34:35], v[60:61] op_sel:[0,1]\n\t" \
  "ds_read_b128 v[104:107], v1 offset:1024\n\t" \
  "v_pk_mul_f32 v[136:137], v[32:33], v[62:63] op_sel_hi:[1,0]\n\t" \
  "v_pk_mul_f32 v[138:139], v[34:35], v[62:63] op_sel_hi:[1,0]\n\t" \
  "ds_read_b128 v[108:111], v1 offset:2048\n\t" \
  "v_pk_mul_f32 v[140:141], v[32:33], v[62:63] op_sel:[0,1]\n\t" \
  "v_pk_mul_f32 v[142:143], v[34:35], v[62:63] op_sel:[0,1]\n\t" \
  "ds_read_b128 v[112:115], v1 offset:3072\n\t" \
  "v_pk_mul_f32 v[144:145], v[36:37], v[60:61] op_sel_hi:[1,0]\n\t" \
  "v_pk_mul_f32 v[146:147], v[38:39], v[60:61] op_sel_hi:[1,0]\n\t" \
  "ds_read_b64_tr_b16 v[64:65], v0 offset:0\n\t" \
  "v_pk_mul_f32 v[148:149], v[36:37], v[60:61] op_sel:[0,1]\n\t" \
  "v_pk_mul_f32 v[150:151], v[38:39], v[60:61] op_sel:[0,1]\n\t" \
  "ds_read_b64_tr_b16 v[66:67], v0 offset:3584\n\t" \
  "v_pk_mul_f32 v[152:153], v[36:37], v[62:63] op_sel_hi:[1,0]\n\t" \
  "v_pk_mul_f32 v[154:155], v[38:39], v[62:63] op_sel_hi:[1,0]\n\t" \
  "ds_read_b64_tr_b16 v[68:69], v0 offset:16\n\t" \
  "v_pk_mul_f32 v[156:157], v[36:37], v[62:63] op_sel:[0,1]\n\t" \
  "v_pk_mul_f32 v[158:159], v[38:39], v[62:63] op_sel:[0,1]\n\t" \
  "ds_read_b64_tr_b16 v[70:71], v0 offset:3600\n\t" \
  "v_pk_mul_f32 v[160:161], v[40:41], v[60:61] op_sel_hi:[1,0]\n\t" \
  "v_pk_mul_f32 v[162:163], v[42:43], v[60:61] op_sel_hi:[1,0]\n\t" \
  "ds_read_b64_tr_b16 v[72:73], v0 offset:32\n\t" \
  "v_pk_mul_f32 v[164:165], v[40:41], v[60:61] op_sel:[0,1]\n\t" \
  "v_pk_mul_f32 v[166:167], v[42:43], v[60:61] op_sel:[0,1]\n\t" \
  "ds_read_b64_tr_b16 v[74:75], v0 offset:3616\n\t" \
  "v_pk_mul_f32 v[168:169], v[40:41], v[62:63] op_sel_hi:[1,0]\n\t" \
  "v_pk_mul_f32 v[170:171], v[42:43], v[62:63] op_sel_hi:[1,0]\n\t" \
  "ds_read_b64_tr_b16 v[76:77], v0 offset:48\n\t" \
  "v_pk_mul_f32 v[172:173], v[40:41], v[62:63] op_sel:[0,1]\n\t" \
  "v_pk_mul_f32 v[174:175], v[42:43], v[62:63] op_sel:[0,1]\n\t" \
  "ds_read_b64_tr_b16 v[78:79], v0 offset:3632\n\t" \
  "v_pk_mul_f32 v[176:177], v[44:45], v[60:61] op_sel_hi:[1,0]\n\t" \
  "v_pk_mul_f32 v[178:179], v[46:47], v[60:61] op_sel_hi:[1,0]\n\t" \
  "ds_read_b64_tr_b16 v[80:81], v0 offset:64\n\t" \
  "v_pk_mul_f32 v[180:181], v[44:45], v[60:61] op_sel:[0,1]\n\t" \
  "v_pk_mul_f32 v[182:183], v[46:47], v[60:61] op_sel:[0,1]\n\t" \
  "ds_read_b64_tr_b16 v[82:83], v0 offset:3648\n\t" \
  "v_pk_mul_f32 v[184:185], v[44:45], v[62:63] op_sel_hi:[1,0]\n\t" \
  "v_pk_mul_f32 v[186:187], v[46:47], v[62:63] op_sel_hi:[1,0]\n\t" \
  "ds_read_b64_tr_b16 v[84:85], v0 offset:80\n\t" \
  "v_pk_mul_f32 v[188:189], v[44:45], v[62:63] op_sel:[0,1]\n\t" \
  "v_pk_mul_f32 v[190:191], v[46:47], v[62:63] op_sel:[0,1]\n\t" \
  "ds_read_b64_tr_b16 v[86:87], v0 offset:3664\n\t" \
  "v_pk_mul_f32 v[192:193], v[48:49], v[60:61] op_sel_hi:[1,0]\n\t" \
  "v_pk_mul_f32 v[194:195], v[50:51], v[60:61] op_sel_hi:[1,0]\n\t" \
  "ds_read_b64_tr_b16 v[88:89], v118 offset:0\n\t" \
  "v_pk_mul_f32 v[196:197], v[48:49], v[60:61] op_sel:[0,1]\n\t" \
  "v_pk_mul_f32 v[198:199], v[50:51], v[60:61] op_sel:[0,1]\n\t" \
  "ds_read_b64_tr_b16 v[90:91], v118 offset:3584\n\t" \
  "v_pk_mul_f32 v[200:201], v[48:49], v[62:63] op_sel_hi:[1,0]\n\t" \
  "v_pk_mul_f32 v[202:203], v[50:51], v[62:63] op_sel_hi:[1,0]\n\t" \
  "v_pk_mul_f32 v[204:205], v[48:49], v[62:63] op_sel:[0,1]\n\t" \
  "v_pk_mul_f32 v[206:207], v[50:51], v[62:63] op_sel:[0,1]\n\t" \
  "v_pk_mul_f32 v[208:209], v[52:53], v[60:61] op_sel_hi:[1,0]\n\t" \
  "v_pk_mul_f32 v[210:211], v[54:55], v[60:61] op_sel_hi:[1,0]\n\t" \
  "v_pk_mul_f32 v[212:213], v[52:53], v[60:61] op_sel:[0,1]\n\t" \
  "v_pk_mul_f32 v[214:215], v[54:55], v[60:61] op_sel:[0,1]\n\t" \
  "v_pk_mul_f32 v[216:217], v[52:53], v[62:63] op_sel_hi:[1,0]\n\t" \
  "v_pk_mul_f32 v[218:219], v[54:55], v[62:63] op_sel_hi:[1,0]\n\t" \
  "v_pk_mul_f32 v[220:221], v[52:53], v[62:63] op_sel:[0,1]\n\t" \
  "v_pk_mul_f32 v[222:223], v[54:55], v[62:63] op_sel:[0,1]\n\t" \
  "v_pk_mul_f32 v[224:225], v[56:57], v[60:61] op_sel_hi:[1,0]\n\t" \
  "v_pk_mul_f32 v[226:227], v[58:59], v[60:61] op_sel_hi:[1,0]\n\t" \
  "v_pk_mul_f32 v[228:229], v[56:57], v[60:61] op_sel:[0,1]\n\t" \
  "v_pk_mul_f32 v[230:231], v[58:59], v[60:61] op_sel:[0,1]\n\t" \
  "v_pk_mul_f32 v[232:233], v[56:57], v[62:63] op_sel_hi:[1,0]\n\t" \
  "v_pk_mul_f32 v[234:235], v[58:59], v[62:63] op_sel_hi:[1,0]\n\t" \
  "v_pk_mul_f32 v[236:237], v[56:57], v[62:63] op_sel:[0,1]\n\t" \
  "v_pk_mul_f32 v[238:239], v[58:59], v[62:63] op_sel:[0,1]\n\t" \
  "s_mov_b32 %[cnt], 14\n\t" \
  "s_waitcnt vmcnt(4) lgkmcnt(0)\n\t" \
  "s_barrier\n\t" \
  "s_branch 2f\n\t" \
  "4:\n\t" \
  "s_waitcnt vmcnt(6)\n\t" \
  "s_barrier\n\t" \
  "s_branch 3f\n\t" \
  "1:\n\t" \
  "s_waitcnt vmcnt(4) lgkmcnt(0)\n\t" \
  "s_barrier\n\t" \
  "2:\n\t" \
  "v_add_u32 v116, %[rd], v0\n\t" \
  "v_add_u32 v117, %[rd], v1\n\t" \
  "s_setprio 3\n\t" \
  "v_mfma_f32_16x16x32_bf16 v[128:131], v[64:67], v[100:103], v[128:131]\n\t" \
  "ds_read_b128 v[240:243], v117 offset:0\n\t" \
  "v_mfma_f32_16x16x32_bf16 v[132:135], v[64:67], v[104:107], v[132:135]\n\t" \
  "ds_read_b128 v[244:247], v117 offset:1024\n\t" \
  "s_add_u32 m0, %[wr], %[dT]\n\t" \
  "v_mfma_f32_16x16x32_bf16 v[136:139], v[64:67], v[108:111], v[136:139]\n\t" \
  "ds_read_b128 v[248:251], v117 offset:2048\n\t" \
  "buffer_load_dwordx4 v2, %[srdY], %[sT] offen sc1 lds\n\t" \
  "v_mfma_f32_16x16x32_bf16 v[140:143], v[64:67], v[112:115], v[140:143]\n\t" \
  "v_add_u32 v118, %[d6], v116\n\t" \
  "ds_read_b64_tr_b16 v[64:65], v116 offset:0\n\t" \
  "v_mfma_f32_16x16x32_bf16 v[156:159], v[68:71], v[112:115], v[156:159]\n\t" \
  "ds_read_b64_tr_b16 v[66:67], v116 offset:3584\n\t" \
  "v_mfma_f32_16x16x32_bf16 v[152:155], v[68:71], v[108:111], v[152:155]\n\t" \
  "ds_read_b128 v[252:255], v117 offset:3072\n\t" \
  "v_mfma_f32_16x16x32_bf16 v[148:151], v[68:71], v[104:107], v[148:151]\n\t" \
  "ds_read_b64_tr_b16 v[92:93], v116 offset:80\n\t" \
  "v_mfma_f32_16x16x32_bf16 v[144:147], v[68:71], v[100:103], v[144:147]\n\t" \
  "ds_read_b64_tr_b16 v[68:69], v116 offset:16\n\t" \
  "v_mfma_f32_16x16x32_bf16 v[160:163], v[72:75], v[100:103], v[160:163]\n\t" \
  "ds_read_b64_tr_b16 v[70:71], v116 offset:3600\n\t" \
  "s_add_u32 m0, %[wr], %[dW]\n\t" \
  "v_mfma_f32_16x16x32_bf16 v[164:167], v[72:75], v[104:107], v[164:167]\n\t" \
  "ds_read_b64_tr_b16 v[94:95], v116 offset:3664\n\t" \
  "buffer_load_dwordx4 v4, %[srdG], %[sW] offen lds\n\t" \
  "v_mfma_f32_16x16x32_bf16 v[168:171], v[72:75], v[108:111], v[168:171]\n\t" \
  "ds_read_b64_tr_b16 v[96:97], v118 offset:0\n\t" \
  "v_mfma_f32_16x16x32_bf16 v[172:175], v[72:75], v[112:115], v[172:175]\n\t" \
  "ds_read_b64_tr_b16 v[72:73], v116 offset:32\n\t" \
  "v_mfma_f32_16x16x32_bf16 v[188:191], v[76:79], v[112:115], v[188:191]\n\t" \
  "ds_read_b64_tr_b16 v[74:75], v116 offset:3616\n\t" \
  "v_mfma_f32_16x16x32_bf16 v[184:187], v[76:79], v[108:111], v[184:187]\n\t" \
  "ds_read_b64_tr_b16 v[98:99], v118 offset:3584\n\t" \
  "s_add_u32 m0, %[wr], %[dT]\n\t" \
  "s_add_u32 m0, m0, 0x400\n\t" \
  "s_setprio 2\n\t" \
  "v_mfma_f32_16x16x32_bf16 v[180:183], v[76:79], v[104:107], v[180:183]\n\t" \
  "buffer_load_dwordx4 v3, %[srdY], %[sT] offen sc1 lds\n\t" \
  "v_mfma_f32_16x16x32_bf16 v[176:179], v[76:79], v[100:103], v[176:179]\n\t" \
  "ds_read_b64_tr_b16 v[76:77], v116 offset:48\n\t" \
  "v_mfma_f32_16x16x32_bf16 v[192:195], v[80:83], v[100:103], v[192:195]\n\t" \
  "ds_read_b64_tr_b16 v[78:79], v116 offset:3632\n\t" \
  "v_mfma_f32_16x16x32_bf16 v[196:199], v[80:83], v[104:107], v[196:199]\n\t" \
  "s_add_u32 m0, %[wr], %[dW]\n\t" \
  "s_add_u32 m0, m0, 7184\n\t" \
  "v_mfma_f32_16x16x32_bf16 v[200:203], v[80:83], v[108:111], v[200:203]\n\t" \
  "buffer_load_dwordx4 v4, %[srdU], %[sW] offen lds\n\t" \
  "v_mfma_f32_16x16x32_bf16 v[204:207], v[80:83], v[112:115], v[204:207]\n\t" \
  "ds_read_b64_tr_b16 v[80:81], v116 offset:64\n\t" \
  "v_mfma_f32_16x16x32_bf16 v[220:223], v[84:87], v[112:115], v[220:223]\n\t" \
  "ds_read_b64_tr_b16 v[82:83], v116 offset:3648\n\t" \
  "v_mfma_f32_16x16x32_bf16 v[216:219], v[84:87], v[108:111], v[216:219]\n\t" \
  "s_add_u32 %[rd], %[rd], 30736\n\t" \
  "s_cmp_eq_u32 %[rd], 153680\n\t" \
  "v_mfma_f32_16x16x32_bf16 v[212:215], v[84:87], v[104:107], v[212:215]\n\t" \
  "s_cselect_b32 %[rd], 0, %[rd]\n\t" \
  "v_mfma_f32_16x16x32_bf16 v[208:211], v[84:87], v[100:103], v[208:211]\n\t" \
  "s_add_u32 %[sT], %[sT], 0x400\n\t" \
  "s_add_u32 %[sW], %[sW], 229376\n\t" \
  "v_mfma_f32_16x16x32_bf16 v[224:227], v[88:91], v[100:103], v[224:227]\n\t" \
  "s_add_u32 %[wr], %[wr], 30736\n\t" \
  "s_cmp_eq_u32 %[wr], 153680\n\t" \
  "v_mfma_f32_16x16x32_bf16 v[228:231], v[88:91], v[104:107], v[228:231]\n\t" \
  "s_cselect_b32 %[wr], 0, %[wr]\n\t" \
  "v_mfma_f32_16x16x32_bf16 v[232:235], v[88:91], v[108:111], v[232:235]\n\t" \
  "v_mfma_f32_16x16x32_bf16 v[236:239], v[88:91], v[112:115], v[236:239]\n\t" \
  "s_cmp_eq_u32 %[cnt], 14\n\t" \
  "s_cbranch_scc1 4b\n\t" \
  "3:\n\t" \
  "s_waitcnt lgkmcnt(0)\n\t" \
  "v_add_u32 v116, %[rd], v0\n\t" \
  "v_add_u32 v117, %[rd], v1\n\t" \
  "s_setprio 1\n\t" \
  "v_mfma_f32_16x16x32_bf16 v[128:131], v[64:67], v[240:243], v[128:131]\n\t" \
  "ds_read_b128 v[100:103], v117 offset:0\n\t" \
  "v_mfma_f32_16x16x32_bf16 v[132:135], v[64:67], v[244:247], v[132:135]\n\t" \
  "ds_read_b128 v[104:107], v117 offset:1024\n\t" \
  "s_add_u32 m0, %[wr], %[dT]\n\t" \
  "v_mfma_f32_16x16x32_bf16 v[136:139], v[64:67], v[248:251], v[136:139]\n\t" \
  "ds_read_b128 v[108:111], v117 offset:2048\n\t" \
  "buffer_load_dwordx4 v2, %[srdY], %[sT] offen sc1 lds\n\t" \
  "v_mfma_f32_16x16x32_bf16 v[140:143], v[64:67], v[252:255], v[140:143]\n\t" \
  "v_add_u32 v118, %[d6], v116\n\t" \
  "ds_read_b64_tr_b16 v[64:65], v116 offset:0\n\t" \
  "v_mfma_f32_16x16x32_bf16 v[156:159], v[68:71], v[252:255], v[156:159]\n\t" \
  "ds_read_b64_tr_b16 v[66:67], v116 offset:3584\n\t" \
  "v_mfma_f32_16x16x32_bf16 v[152:155], v[68:71], v[248:251], v[152:155]\n\t" \
  "ds_read_b128 v[112:115], v117 offset:3072\n\t" \
  "v_mfma_f32_16x16x32_bf16 v[148:151], v[68:71], v[244:247], v[148:151]\n\t" \
  "ds_read_b64_tr_b16 v[84:85], v116 offset:80\n\t" \
  "v_mfma_f32_16x16x32_bf16 v[144:147], v[68:71], v[240:243], v[144:147]\n\t" \
  "ds_read_b64_tr_b16 v[68:69], v116 offset:16\n\t" \
  "v_mfma_f32_16x16x32_bf16 v[160:163], v[72:75], v[240:243], v[160:163]\n\t" \
  "ds_read_b64_tr_b16 v[70:71], v116 offset:3600\n\t" \
  "s_add_u32 m0, %[wr], %[dW]\n\t" \
  "v_mfma_f32_16x16x32_bf16 v[164:167], v[72:75], v[244:247], v[164:167]\n\t" \
  "ds_read_b64_tr_b16 v[86:87], v116 offset:3664\n\t" \
  "buffer_load_dwordx4 v4, %[srdG], %[sW] offen lds\n\t" \
  "v_mfma_f32_16x16x32_bf16 v[168:171], v[72:75], v[248:251], v[168:171]\n\t" \
  "ds_read_b64_tr_b16 v[88:89], v118 offset:0\n\t" \
  "v_mfma_f32_16x16x32_bf16 v[172:175], v[72:75], v[252:255], v[172:175]\n\t" \
  "ds_read_b64_tr_b16 v[72:73], v116 offset:32\n\t" \
  "v_mfma_f32_16x16x32_bf16 v[188:191], v[76:79], v[252:255], v[188:191]\n\t" \
  "ds_read_b64_tr_b16 v[74:75], v116 offset:3616\n\t" \
  "v_mfma_f32_16x16x32_bf16 v[184:187], v[76:79], v[248:251], v[184:187]\n\t" \
  "ds_read_b64_tr_b16 v[90:91], v118 offset:3584\n\t" \
  "s_add_u32 m0, %[wr], %[dT]\n\t" \
  "s_add_u32 m0, m0, 0x400\n\t" \
  "s_setprio 0\n\t" \
  "v_mfma_f32_16x16x32_bf16 v[180:183], v[76:79], v[244:247], v[180:183]\n\t" \
  "buffer_load_dwordx4 v3, %[srdY], %[sT] offen sc1 lds\n\t" \
  "v_mfma_f32_16x16x32_bf16 v[176:179], v[76:79], v[240:243], v[176:179]\n\t" \
  "ds_read_b64_tr_b16 v[76:77], v116 offset:48\n\t" \
  "v_mfma_f32_16x16x32_bf16 v[192:195], v[80:83], v[240:243], v[192:195]\n\t" \
  "ds_read_b64_tr_b16 v[78:79], v116 offset:3632\n\t" \
  "v_mfma_f32_16x16x32_bf16 v[196:199], v[80:83], v[244:247], v[196:199]\n\t" \
  "s_add_u32 m0, %[wr], %[dW]\n\t" \
  "s_add_u32 m0, m0, 7184\n\t" \
  "v_mfma_f32_16x16x32_bf16 v[200:203], v[80:83], v[248:251], v[200:203]\n\t" \
  "buffer_load_dwordx4 v4, %[srdU], %[sW] offen lds\n\t" \
  "v_mfma_f32_16x16x32_bf16 v[204:207], v[80:83], v[252:255], v[204:207]\n\t" \
  "ds_read_b64_tr_b16 v[80:81], v116 offset:64\n\t" \
  "v_mfma_f32_16x16x32_bf16 v[220:223], v[92:95], v[252:255], v[220:223]\n\t" \
  "ds_read_b64_tr_b16 v[82:83], v116 offset:3648\n\t" \
  "v_mfma_f32_16x16x32_bf16 v[216:219], v[92:95], v[248:251], v[216:219]\n\t" \
  "s_add_u32 %[rd], %[rd], 30736\n\t" \
  "s_cmp_eq_u32 %[rd], 153680\n\t" \
  "v_mfma_f32_16x16x32_bf16 v[212:215], v[92:95], v[244:247], v[212:215]\n\t" \
  "s_cselect_b32 %[rd], 0, %[rd]\n\t" \
  "v_mfma_f32_16x16x32_bf16 v[208:211], v[92:95], v[240:243], v[208:211]\n\t" \
  "s_add_u32 %[sT], %[sT], 0x400\n\t" \
  "s_add_u32 %[sW], %[sW], 229376\n\t" \
  "v_mfma_f32_16x16x32_bf16 v[224:227], v[96:99], v[240:243], v[224:227]\n\t" \
  "s_add_u32 %[wr], %[wr], 30736\n\t" \
  "s_cmp_eq_u32 %[wr], 153680\n\t" \
  "v_mfma_f32_16x16x32_bf16 v[228:231], v[96:99], v[244:247], v[228:231]\n\t" \
  "s_cselect_b32 %[wr], 0, %[wr]\n\t" \
  "v_mfma_f32_16x16x32_bf16 v[232:235], v[96:99], v[248:251], v[232:235]\n\t" \
  "v_mfma_f32_16x16x32_bf16 v[236:239], v[96:99], v[252:255], v[236:239]\n\t" \
  "s_sub_u32 %[cnt], %[cnt], 1\n\t" \
  "s_cmp_lg_u32 %[cnt], 0\n\t" \
  "s_cbranch_scc1 1b\n\t" \
  "s_setprio 0\n\t" \
  "s_waitcnt vmcnt(4) lgkmcnt(0)\n\t" \
  "s_barrier\n\t" \
  "ds_read_b32 v46, v5\n\t" \
  "ds_read_b32 v47, v5 offset:128\n\t" \
  "v_add_u32 v116, 92208, v0\n\t" \
  "v_add_u32 v120, %[d6], v116\n\t" \
  "v_add_u32 v117, 122944, v0\n\t" \
  "v_add_u32 v121, %[d6], v117\n\t" \
  "v_add_u32 v118, 0, v0\n\t" \
  "v_add_u32 v122, %[d6], v118\n\t" \
  "v_add_u32 v119, 30736, v0\n\t" \
  "v_add_u32 v123, %[d6], v119\n\t" \
  "v_add_u32 v14, %[e6], v6\n\t" \
  "v_mfma_f32_16x16x32_bf16 v[128:131], v[64:67], v[100:103], v[128:131]\n\t" \
  "v_add_u32 v15, 122944, v1\n\t" \
  "ds_read_b128 v[240:243], v15\n\t" \
  "ds_read_b128 v[244:247], v15 offset:1024\n\t" \
  "v_mfma_f32_16x16x32_bf16 v[132:135], v[64:67], v[104:107], v[132:135]\n\t" \
  "v_mfma_f32_16x16x32_bf16 v[148:151], v[68:71], v[104:107], v[148:151]\n\t" \
  "v_add_u32 v15, 0, v1\n\t" \
  "ds_read_b128 v[248:251], v15\n\t" \
  "ds_read_b128 v[252:255], v15 offset:1024\n\t" \
  "v_mfma_f32_16x16x32_bf16 v[144:147], v[68:71], v[100:103], v[144:147]\n\t" \
  "v_mfma_f32_16x16x32_bf16 v[160:163], v[72:75], v[100:103], v[160:163]\n\t" \
  "v_mfma_f32_16x16x32_bf16 v[164:167], v[72:75], v[104:107], v[164:167]\n\t" \
  "ds_read_b64_tr_b16 v[68:69], v117 offset:0\n\t" \
  "ds_read_b64_tr_b16 v[70:71], v117 offset:3584\n\t" \
  "v_mfma_f32_16x16x32_bf16 v[180:183], v[76:79], v[104:107], v[180:183]\n\t" \
  "v_mfma_f32_16x16x32_bf16 v[176:179], v[76:79], v[100:103], v[176:179]\n\t" \
  "ds_read_b64_tr_b16 v[72:73], v118 offset:0\n\t" \
  "ds_read_b64_tr_b16 v[74:75], v118 offset:3584\n\t" \
  "v_mfma_f32_16x16x32_bf16 v[192:195], v[80:83], v[100:103], v[192:195]\n\t" \
  "v_mfma_f32_16x16x32_bf16 v[196:199], v[80:83], v[104:107], v[196:199]\n\t" \
  "v_mfma_f32_16x16x32_bf16 v[212:215], v[84:87], v[104:107], v[212:215]\n\t" \
  "v_mfma_f32_16x16x32_bf16 v[208:211], v[84:87], v[100:103], v[208:211]\n\t" \
  "v_mfma_f32_16x16x32_bf16 v[224:227], v[88:91], v[100:103], v[224:227]\n\t" \
  "v_mfma_f32_16x16x32_bf16 v[228:231], v[88:91], v[104:107], v[228:231]\n\t" \
  "s_waitcnt vmcnt(0) lgkmcnt(0)\n\t" \
  "s_barrier\n\t" \
  "ds_read_b64_tr_b16 v[76:77], v119 offset:0\n\t" \
  "ds_read_b64_tr_b16 v[78:79], v119 offset:3584\n\t" \
  "v_add_u32 v15, 30736, v1\n\t" \
  "ds_read_b128 v[48:51], v15\n\t" \
  "ds_read_b128 v[52:55], v15 offset:1024\n\t" \
  "v_mfma_f32_16x16x32_bf16 v[128:131], v[68:71], v[240:243], v[128:131]\n\t" \
  "ds_read_b64_tr_b16 v[84:85], v117 offset:16\n\t" \
  "ds_read_b64_tr_b16 v[86:87], v117 offset:3600\n\t" \
  "v_mfma_f32_16x16x32_bf16 v[132:135], v[68:71], v[244:247], v[132:135]\n\t" \
  "ds_read_b64_tr_b16 v[88:89], v118 offset:16\n\t" \
  "ds_read_b64_tr_b16 v[90:91], v118 offset:3600\n\t" \
  "v_mfma_f32_16x16x32_bf16 v[132:135], v[72:75], v[252:255], v[132:135]\n\t" \
  "ds_read_b64_tr_b16 v[92:93], v119 offset:16\n\t" \
  "ds_read_b64_tr_b16 v[94:95], v119 offset:3600\n\t" \
  "v_mfma_f32_16x16x32_bf16 v[128:131], v[72:75], v[248:251], v[128:131]\n\t" \
  "s_waitcnt lgkmcnt(4)\n\t" \
  "v_mfma_f32_16x16x32_bf16 v[128:131], v[76:79], v[48:51], v[128:131]\n\t" \
  "ds_read_b128 v[96:99], v6 offset:0\n\t" \
  "ds_read_b128 v[124:127], v6 offset:448\n\t" \
  "v_mfma_f32_16x16x32_bf16 v[132:135], v[76:79], v[52:55], v[132:135]\n\t" \
  "s_waitcnt lgkmcnt(0)\n\t" \
  "v_mfma_f32_16x16x32_bf16 v[144:147], v[84:87], v[240:243], v[144:147]\n\t" \
  "ds_read_b64_tr_b16 v[68:69], v117 offset:32\n\t" \
  "ds_read_b64_tr_b16 v[70:71], v117 offset:3616\n\t" \
  "v_add_u32 v15, 124992, v1\n\t" \
  "ds_read_b128 v[56:59], v15\n\t" \
  "ds_read_b128 v[60:63], v15 offset:1024\n\t" \
  "v_mfma_f32_16x16x32_bf16 v[148:151], v[84:87], v[244:247], v[148:151]\n\t" \
  "ds_read_b64_tr_b16 v[72:73], v118 offset:32\n\t" \
  "ds_read_b64_tr_b16 v[74:75], v118 offset:3616\n\t" \
  "v_permlane16_swap_b32 v128, v132\n\t" \
  "v_permlane16_swap_b32 v129, v133\n\t" \
  "v_permlane16_swap_b32 v130, v134\n\t" \
  "v_permlane16_swap_b32 v131, v135\n\t" \
  "v_fma_f32 v128, v128, v46, v96\n\t" \
  "v_mfma_f32_16x16x32_bf16 v[148:151], v[88:91], v[252:255], v[148:151]\n\t" \
  "ds_read_b64_tr_b16 v[76:77], v119 offset:32\n\t" \
  "ds_read_b64_tr_b16 v[78:79], v119 offset:3616\n\t" \
  "v_fma_f32 v129, v129, v46, v97\n\t" \
  "v_fma_f32 v130, v130, v46, v98\n\t" \
  "v_fma_f32 v131, v131, v46, v99\n\t" \
  "v_fma_f32 v132, v132, v46, v124\n\t" \
  "v_fma_f32 v133, v133, v46, v125\n\t" \
  "v_mfma_f32_16x16x32_bf16 v[144:147], v[88:91], v[248:251], v[144:147]\n\t" \
  "v_fma_f32 v134, v134, v46, v126\n\t" \
  "v_fma_f32 v135, v135, v46, v127\n\t" \
  "v_maximum3_f32 v128, v128, 0, 0\n\t" \
  "v_maximum3_f32 v129, v129, 0, 0\n\t" \
  "v_mfma_f32_16x16x32_bf16 v[144:147], v[92:95], v[48:51], v[144:147]\n\t" \
  "v_maximum3_f32 v130, v130, 0, 0\n\t" \
  "v_maximum3_f32 v131, v131, 0, 0\n\t" \
  "v_mul_f32 v128, v128, v132\n\t" \
  "v_mul_f32 v129, v129, v133\n\t" \
  "ds_read_b128 v[96:99], v6 offset:32\n\t" \
  "ds_read_b128 v[124:127], v6 offset:480\n\t" \
  "v_mfma_f32_16x16x32_bf16 v[148:151], v[92:95], v[52:55], v[148:151]\n\t" \
  "v_mul_f32 v130, v130, v134\n\t" \
  "v_mul_f32 v131, v131, v135\n\t" \
  "v_cvt_pk_bf16_f32 v32, v128, v129\n\t" \
  "v_cvt_pk_bf16_f32 v33, v130, v131\n\t" \
  "s_waitcnt lgkmcnt(0)\n\t" \
  "v_mfma_f32_16x16x32_bf16 v[160:163], v[68:71], v[240:243], v[160:163]\n\t" \
  "ds_read_b64_tr_b16 v[84:85], v117 offset:48\n\t" \
  "ds_read_b64_tr_b16 v[86:87], v117 offset:3632\n\t" \
  "v_add_u32 v15, 2048, v1\n\t" \
  "ds_read_b128 v[100:103], v15\n\t" \
  "ds_read_b128 v[104:107], v15 offset:1024\n\t" \
  "v_mfma_f32_16x16x32_bf16 v[164:167], v[68:71], v[244:247], v[164:167]\n\t" \
  "ds_read_b64_tr_b16 v[88:89], v118 offset:48\n\t" \
  "ds_read_b64_tr_b16 v[90:91], v118 offset:3632\n\t" \
  "v_permlane16_swap_b32 v144, v148\n\t" \
  "v_permlane16_swap_b32 v145, v149\n\t" \
  "v_permlane16_swap_b32 v146, v150\n\t" \
  "v_permlane16_swap_b32 v147, v151\n\t" \
  "v_fma_f32 v144, v144, v46, v96\n\t" \
  "v_mfma_f32_16x16x32_bf16 v[164:167], v[72:75], v[252:255], v[164:167]\n\t" \
  "ds_read_b64_tr_b16 v[92:93], v119 offset:48\n\t" \
  "ds_read_b64_tr_b16 v[94:95], v119 offset:3632\n\t" \
  "v_fma_f32 v145, v145, v46, v97\n\t" \
  "v_fma_f32 v146, v146, v46, v98\n\t" \
  "v_fma_f32 v147, v147, v46, v99\n\t" \
  "v_fma_f32 v148, v148, v46, v124\n\t" \
  "v_fma_f32 v149, v149, v46, v125\n\t" \
  "v_mfma_f32_16x16x32_bf16 v[160:163], v[72:75], v[248:251], v[160:163]\n\t" \
  "v_fma_f32 v150, v150, v46, v126\n\t" \
  "v_fma_f32 v151, v151, v46, v127\n\t" \
  "v_maximum3_f32 v144, v144, 0, 0\n\t" \
  "v_maximum3_f32 v145, v145, 0, 0\n\t" \
  "v_maximum3_f32 v146, v146, 0, 0\n\t" \
  "v_mfma_f32_16x16x32_bf16 v[160:163], v[76:79], v[48:51], v[160:163]\n\t" \
  "v_maximum3_f32 v147, v147, 0, 0\n\t" \
  "v_mul_f32 v144, v144, v148\n\t" \
  "v_mul_f32 v145, v145, v149\n\t" \
  "v_mul_f32 v146, v146, v150\n\t" \
  "ds_read_b128 v[96:99], v6 offset:64\n\t" \
  "ds_read_b128 v[124:127], v6 offset:512\n\t" \
  "v_mfma_f32_16x16x32_bf16 v[164:167], v[76:79], v[52:55], v[164:167]\n\t" \
  "v_mul_f32 v147, v147, v151\n\t" \
  "v_cvt_pk_bf16_f32 v34, v144, v145\n\t" \
  "v_cvt_pk_bf16_f32 v35, v146, v147\n\t" \
  "buffer_store_dwordx4 v[32:35], v7, %[srdA], %[oP0] offen offset:0 sc1\n\t" \
  "s_waitcnt lgkmcnt(0)\n\t" \
  "v_mfma_f32_16x16x32_bf16 v[176:179], v[84:87], v[240:243], v[176:179]\n\t" \
  "ds_read_b64_tr_b16 v[68:69], v117 offset:64\n\t" \
  "ds_read_b64_tr_b16 v[70:71], v117 offset:3648\n\t" \
  "v_mfma_f32_16x16x32_bf16 v[180:183], v[84:87], v[244:247], v[180:183]\n\t" \
  "ds_read_b64_tr_b16 v[72:73], v118 offset:64\n\t" \
  "ds_read_b64_tr_b16 v[74:75], v118 offset:3648\n\t" \
  "s_nop 1\n\t" \
  "v_permlane16_swap_b32 v160, v164\n\t" \
  "v_permlane16_swap_b32 v161, v165\n\t" \
  "v_permlane16_swap_b32 v162, v166\n\t" \
  "v_permlane16_swap_b32 v163, v167\n\t" \
  "v_fma_f32 v160, v160, v46, v96\n\t" \
  "v_mfma_f32_16x16x32_bf16 v[180:183], v[88:91], v[252:255], v[180:183]\n\t" \
  "ds_read_b64_tr_b16 v[76:77], v119 offset:64\n\t" \
  "ds_read_b64_tr_b16 v[78:79], v119 offset:3648\n\t" \
  "v_fma_f32 v161, v161, v46, v97\n\t" \
  "v_fma_f32 v162, v162, v46, v98\n\t" \
  "v_fma_f32 v163, v163, v46, v99\n\t" \
  "v_fma_f32 v164, v164, v46, v124\n\t" \
  "v_fma_f32 v165, v165, v46, v125\n\t" \
  "v_mfma_f32_16x16x32_bf16 v[176:179], v[88:91], v[248:251], v[176:179]\n\t" \
  "v_fma_f32 v166, v166, v46, v126\n\t" \
  "v_fma_f32 v167, v167, v46, v127\n\t" \
  "v_maximum3_f32 v160, v160, 0, 0\n\t" \
  "v_maximum3_f32 v161, v161, 0, 0\n\t" \
  "v_mfma_f32_16x16x32_bf16 v[176:179], v[92:95], v[48:51], v[176:179]\n\t" \
  "v_maximum3_f32 v162, v162, 0, 0\n\t" \
  "v_maximum3_f32 v163, v163, 0, 0\n\t" \
  "v_mul_f32 v160, v160, v164\n\t" \
  "v_mul_f32 v161, v161, v165\n\t" \
  "ds_read_b128 v[96:99], v6 offset:96\n\t" \
  "ds_read_b128 v[124:127], v6 offset:544\n\t" \
  "v_mfma_f32_16x16x32_bf16 v[180:183], v[92:95], v[52:55], v[180:183]\n\t" \
  "v_mul_f32 v162, v162, v166\n\t" \
  "v_mul_f32 v163, v163, v167\n\t" \
  "v_cvt_pk_bf16_f32 v36, v160, v161\n\t" \
  "v_cvt_pk_bf16_f32 v37, v162, v163\n\t" \
  "s_waitcnt lgkmcnt(0)\n\t" \
  "v_mfma_f32_16x16x32_bf16 v[192:195], v[68:71], v[240:243], v[192:195]\n\t" \
  "ds_read_b64_tr_b16 v[84:85], v117 offset:80\n\t" \
  "ds_read_b64_tr_b16 v[86:87], v117 offset:3664\n\t" \
  "v_mfma_f32_16x16x32_bf16 v[196:199], v[68:71], v[244:247], v[196:199]\n\t" \
  "ds_read_b64_tr_b16 v[88:89], v118 offset:80\n\t" \
  "ds_read_b64_tr_b16 v[90:91], v118 offset:3664\n\t" \
  "s_nop 1\n\t" \
  "v_permlane16_swap_b32 v176, v180\n\t" \
  "v_permlane16_swap_b32 v177, v181\n\t" \
  "v_permlane16_swap_b32 v178, v182\n\t" \
  "v_permlane16_swap_b32 v179, v183\n\t" \
  "v_fma_f32 v176, v176, v46, v96\n\t" \
  "v_mfma_f32_16x16x32_bf16 v[196:199], v[72:75], v[252:255], v[196:199]\n\t" \
  "ds_read_b64_tr_b16 v[92:93], v119 offset:80\n\t" \
  "ds_read_b64_tr_b16 v[94:95], v119 offset:3664\n\t" \
  "v_fma_f32 v177, v177, v46, v97\n\t" \
  "v_fma_f32 v178, v178, v46, v98\n\t" \
  "v_fma_f32 v179, v179, v46, v99\n\t" \
  "v_fma_f32 v180, v180, v46, v124\n\t" \
  "v_fma_f32 v181, v181, v46, v125\n\t" \
  "v_mfma_f32_16x16x32_bf16 v[192:195], v[72:75], v[248:251], v[192:195]\n\t" \
  "v_fma_f32 v182, v182, v46, v126\n\t" \
  "v_fma_f32 v183, v183, v46, v127\n\t" \
  "v_maximum3_f32 v176, v176, 0, 0\n\t" \
  "v_maximum3_f32 v177, v177, 0, 0\n\t" \
  "v_maximum3_f32 v178, v178, 0, 0\n\t" \
  "v_mfma_f32_16x16x32_bf16 v[192:195], v[76:79], v[48:51], v[192:195]\n\t" \
  "v_maximum3_f32 v179, v179, 0, 0\n\t" \
  "v_mul_f32 v176, v176, v180\n\t" \
  "v_mul_f32 v177, v177, v181\n\t" \
  "v_mul_f32 v178, v178, v182\n\t" \
  "ds_read_b128 v[96:99], v6 offset:128\n\t" \
  "ds_read_b128 v[124:127], v6 offset:576\n\t" \
  "v_mfma_f32_16x16x32_bf16 v[196:199], v[76:79], v[52:55], v[196:199]\n\t" \
  "v_mul_f32 v179, v179, v183\n\t" \
  "v_cvt_pk_bf16_f32 v38, v176, v177\n\t" \
  "v_cvt_pk_bf16_f32 v39, v178, v179\n\t" \
  "buffer_store_dwordx4 v[36:39], v7, %[srdA], %[oP0] offen offset:512 sc1\n\t" \
  "s_waitcnt lgkmcnt(0)\n\t" \
  "v_mfma_f32_16x16x32_bf16 v[208:211], v[84:87], v[240:243], v[208:211]\n\t" \
  "ds_read_b64_tr_b16 v[68:69], v121 offset:0\n\t" \
  "ds_read_b64_tr_b16 v[70:71], v121 offset:3584\n\t" \
  "v_mfma_f32_16x16x32_bf16 v[212:215], v[84:87], v[244:247], v[212:215]\n\t" \
  "ds_read_b64_tr_b16 v[72:73], v122 offset:0\n\t" \
  "ds_read_b64_tr_b16 v[74:75], v122 offset:3584\n\t" \
  "s_nop 1\n\t" \
  "v_permlane16_swap_b32 v192, v196\n\t" \
  "v_permlane16_swap_b32 v193, v197\n\t" \
  "v_permlane16_swap_b32 v194, v198\n\t" \
  "v_permlane16_swap_b32 v195, v199\n\t" \
  "v_fma_f32 v192, v192, v46, v96\n\t" \
  "v_mfma_f32_16x16x32_bf16 v[212:215], v[88:91], v[252:255], v[212:215]\n\t" \
  "ds_read_b64_tr_b16 v[76:77], v123 offset:0\n\t" \
  "ds_read_b64_tr_b16 v[78:79], v123 offset:3584\n\t" \
  "v_fma_f32 v193, v193, v46, v97\n\t" \
  "v_fma_f32 v194, v194, v46, v98\n\t" \
  "v_fma_f32 v195, v195, v46, v99\n\t" \
  "v_fma_f32 v196, v196, v46, v124\n\t" \
  "v_fma_f32 v197, v197, v46, v125\n\t" \
  "v_mfma_f32_16x16x32_bf16 v[208:211], v[88:91], v[248:251], v[208:211]\n\t" \
  "v_fma_f32 v198, v198, v46, v126\n\t" \
  "v_fma_f32 v199, v199, v46, v127\n\t" \
  "v_maximum3_f32 v192, v192, 0, 0\n\t" \
  "v_maximum3_f32 v193, v193, 0, 0\n\t" \
  "v_mfma_f32_16x16x32_bf16 v[208:211], v[92:95], v[48:51], v[208:211]\n\t" \
  "v_maximum3_f32 v194, v194, 0, 0\n\t" \
  "v_maximum3_f32 v195, v195, 0, 0\n\t" \
  "v_mul_f32 v192, v192, v196\n\t" \
  "v_mul_f32 v193, v193, v197\n\t" \
  "ds_read_b128 v[96:99], v6 offset:160\n\t" \
  "ds_read_b128 v[124:127], v6 offset:608\n\t" \
  "v_mfma_f32_16x16x32_bf16 v[212:215], v[92:95], v[52:55], v[212:215]\n\t" \
  "v_mul_f32 v194, v194, v198\n\t" \
  "v_mul_f32 v195, v195, v199\n\t" \
  "v_cvt_pk_bf16_f32 v40, v192, v193\n\t" \
  "v_cvt_pk_bf16_f32 v41, v194, v195\n\t" \
  "s_waitcnt lgkmcnt(0)\n\t" \
  "v_mfma_f32_16x16x32_bf16 v[224:227], v[68:71], v[240:243], v[224:227]\n\t" \
  "ds_read_b64_tr_b16 v[80:81], v116 offset:0\n\t" \
  "ds_read_b64_tr_b16 v[82:83], v116 offset:3584\n\t" \
  "v_mfma_f32_16x16x32_bf16 v[228:231], v[68:71], v[244:247], v[228:231]\n\t" \
  "ds_read_b64_tr_b16 v[84:85], v117 offset:0\n\t" \
  "ds_read_b64_tr_b16 v[86:87], v117 offset:3584\n\t" \
  "s_nop 1\n\t" \
  "v_permlane16_swap_b32 v208, v212\n\t" \
  "v_permlane16_swap_b32 v209, v213\n\t" \
  "v_permlane16_swap_b32 v210, v214\n\t" \
  "v_permlane16_swap_b32 v211, v215\n\t" \
  "v_fma_f32 v208, v208, v46, v96\n\t" \
  "v_mfma_f32_16x16x32_bf16 v[228:231], v[72:75], v[252:255], v[228:231]\n\t" \
  "ds_read_b64_tr_b16 v[88:89], v118 offset:0\n\t" \
  "ds_read_b64_tr_b16 v[90:91], v118 offset:3584\n\t" \
  "v_fma_f32 v209, v209, v46, v97\n\t" \
  "v_fma_f32 v210, v210, v46, v98\n\t" \
  "v_fma_f32 v211, v211, v46, v99\n\t" \
  "v_fma_f32 v212, v212, v46, v124\n\t" \
  "v_fma_f32 v213, v213, v46, v125\n\t" \
  "v_mfma_f32_16x16x32_bf16 v[224:227], v[72:75], v[248:251], v[224:227]\n\t" \
  "ds_read_b64_tr_b16 v[92:93], v119 offset:0\n\t" \
  "ds_read_b64_tr_b16 v[94:95], v119 offset:3584\n\t" \
  "v_fma_f32 v214, v214, v46, v126\n\t" \
  "v_fma_f32 v215, v215, v46, v127\n\t" \
  "v_maximum3_f32 v208, v208, 0, 0\n\t" \
  "v_maximum3_f32 v209, v209, 0, 0\n\t" \
  "v_maximum3_f32 v210, v210, 0, 0\n\t" \
  "v_mfma_f32_16x16x32_bf16 v[224:227], v[76:79], v[48:51], v[224:227]\n\t" \
  "v_maximum3_f32 v211, v211, 0, 0\n\t" \
  "v_mul_f32 v208, v208, v212\n\t" \
  "v_mul_f32 v209, v209, v213\n\t" \
  "v_mul_f32 v210, v210, v214\n\t" \
  "ds_read_b128 v[96:99], v14 offset:0\n\t" \
  "ds_read_b128 v[124:127], v14 offset:448\n\t" \
  "v_mfma_f32_16x16x32_bf16 v[228:231], v[76:79], v[52:55], v[228:231]\n\t" \
  "v_mul_f32 v211, v211, v215\n\t" \
  "v_cvt_pk_bf16_f32 v42, v208, v209\n\t" \
  "v_cvt_pk_bf16_f32 v43, v210, v211\n\t" \
  "buffer_store_dwordx4 v[40:43], v7, %[srdA], %[oP0] offen offset:1024 sc1\n\t" \
  "s_waitcnt lgkmcnt(0)\n\t" \
  "v_mfma_f32_16x16x32_bf16 v[136:139], v[80:83], v[108:111], v[136:139]\n\t" \
  "ds_read_b64_tr_b16 v[64:65], v116 offset:16\n\t" \
  "ds_read_b64_tr_b16 v[66:67], v116 offset:3600\n\t" \
  "v_mfma_f32_16x16x32_bf16 v[140:143], v[80:83], v[112:115], v[140:143]\n\t" \
  "ds_read_b64_tr_b16 v[68:69], v117 offset:16\n\t" \
  "ds_read_b64_tr_b16 v[70:71], v117 offset:3600\n\t" \
  "s_nop 1\n\t" \
  "v_permlane16_swap_b32 v224, v228\n\t" \
  "v_permlane16_swap_b32 v225, v229\n\t" \
  "v_permlane16_swap_b32 v226, v230\n\t" \
  "v_permlane16_swap_b32 v227, v231\n\t" \
  "v_add_u32 v15, 32784, v1\n\t" \
  "ds_read_b128 v[240:243], v15\n\t" \
  "ds_read_b128 v[244:247], v15 offset:1024\n\t" \
  "v_mfma_f32_16x16x32_bf16 v[140:143], v[84:87], v[60:63], v[140:143]\n\t" \
  "ds_read_b64_tr_b16 v[72:73], v118 offset:16\n\t" \
  "ds_read_b64_tr_b16 v[74:75], v118 offset:3600\n\t" \
  "v_fma_f32 v224, v224, v46, v96\n\t" \
  "v_fma_f32 v225, v225, v46, v97\n\t" \
  "v_fma_f32 v226, v226, v46, v98\n\t" \
  "v_fma_f32 v227, v227, v46, v99\n\t" \
  "v_mfma_f32_16x16x32_bf16 v[136:139], v[84:87], v[56:59], v[136:139]\n\t" \
  "ds_read_b64_tr_b16 v[76:77], v119 offset:16\n\t" \
  "ds_read_b64_tr_b16 v[78:79], v119 offset:3600\n\t" \
  "v_fma_f32 v228, v228, v46, v124\n\t" \
  "v_fma_f32 v229, v229, v46, v125\n\t" \
  "v_fma_f32 v230, v230, v46, v126\n\t" \
  "v_mfma_f32_16x16x32_bf16 v[136:139], v[88:91], v[100:103], v[136:139]\n\t" \
  "v_fma_f32 v231, v231, v46, v127\n\t" \
  "v_maximum3_f32 v224, v224, 0, 0\n\t" \
  "v_maximum3_f32 v225, v225, 0, 0\n\t" \
  "v_mfma_f32_16x16x32_bf16 v[140:143], v[88:91], v[104:107], v[140:143]\n\t" \
  "v_maximum3_f32 v226, v226, 0, 0\n\t" \
  "v_maximum3_f32 v227, v227, 0, 0\n\t" \
  "v_mul_f32 v224, v224, v228\n\t" \
  "s_waitcnt lgkmcnt(2)\n\t" \
  "v_mfma_f32_16x16x32_bf16 v[140:143], v[92:95], v[244:247], v[140:143]\n\t" \
  "v_mul_f32 v225, v225, v229\n\t" \
  "v_mul_f32 v226, v226, v230\n\t" \
  "v_mul_f32 v227, v227, v231\n\t" \
  "ds_read_b128 v[96:99], v6 offset:0\n\t" \
  "ds_read_b128 v[124:127], v6 offset:448\n\t" \
  "v_mfma_f32_16x16x32_bf16 v[136:139], v[92:95], v[240:243], v[136:139]\n\t" \
  "v_cvt_pk_bf16_f32 v44, v224, v225\n\t" \
  "v_cvt_pk_bf16_f32 v45, v226, v227\n\t" \
  "buffer_store_dwordx2 v[44:45], v7, %[srdA], %[oL0] offen sc1\n\t" \
  "s_waitcnt lgkmcnt(0)\n\t" \
  "v_mfma_f32_16x16x32_bf16 v[152:155], v[64:67], v[108:111], v[152:155]\n\t" \
  "ds_read_b64_tr_b16 v[80:81], v116 offset:32\n\t" \
  "ds_read_b64_tr_b16 v[82:83], v116 offset:3616\n\t" \
  "v_mfma_f32_16x16x32_bf16 v[156:159], v[64:67], v[112:115], v[156:159]\n\t" \
  "ds_read_b64_tr_b16 v[84:85], v117 offset:32\n\t" \
  "ds_read_b64_tr_b16 v[86:87], v117 offset:3616\n\t" \
  "s_nop 1\n\t" \
  "v_permlane16_swap_b32 v136, v140\n\t" \
  "v_permlane16_swap_b32 v137, v141\n\t" \
  "v_permlane16_swap_b32 v138, v142\n\t" \
  "v_permlane16_swap_b32 v139, v143\n\t" \
  "v_mfma_f32_16x16x32_bf16 v[156:159], v[68:71], v[60:63], v[156:159]\n\t" \
  "ds_read_b64_tr_b16 v[88:89], v118 offset:32\n\t" \
  "ds_read_b64_tr_b16 v[90:91], v118 offset:3616\n\t" \
  "v_fma_f32 v136, v136, v47, v96\n\t" \
  "v_fma_f32 v137, v137, v47, v97\n\t" \
  "v_fma_f32 v138, v138, v47, v98\n\t" \
  "v_mfma_f32_16x16x32_bf16 v[152:155], v[68:71], v[56:59], v[152:155]\n\t" \
  "ds_read_b64_tr_b16 v[92:93], v119 offset:32\n\t" \
  "ds_read_b64_tr_b16 v[94:95], v119 offset:3616\n\t" \
  "v_fma_f32 v139, v139, v47, v99\n\t" \
  "v_fma_f32 v140, v140, v47, v124\n\t" \
  "v_fma_f32 v141, v141, v47, v125\n\t" \
  "v_mfma_f32_16x16x32_bf16 v[152:155], v[72:75], v[100:103], v[152:155]\n\t" \
  "v_fma_f32 v142, v142, v47, v126\n\t" \
  "v_fma_f32 v143, v143, v47, v127\n\t" \
  "v_maximum3_f32 v136, v136, 0, 0\n\t" \
  "v_mfma_f32_16x16x32_bf16 v[156:159], v[72:75], v[104:107], v[156:159]\n\t" \
  "v_maximum3_f32 v137, v137, 0, 0\n\t" \
  "v_maximum3_f32 v138, v138, 0, 0\n\t" \
  "v_maximum3_f32 v139, v139, 0, 0\n\t" \
  "v_mfma_f32_16x16x32_bf16 v[156:159], v[76:79], v[244:247], v[156:159]\n\t" \
  "v_mul_f32 v136, v136, v140\n\t" \
  "v_mul_f32 v137, v137, v141\n\t" \
  "v_mul_f32 v138, v138, v142\n\t" \
  "ds_read_b128 v[96:99], v6 offset:32\n\t" \
  "ds_read_b128 v[124:127], v6 offset:480\n\t" \
  "v_mfma_f32_16x16x32_bf16 v[152:155], v[76:79], v[240:243], v[152:155]\n\t" \
  "v_mul_f32 v139, v139, v143\n\t" \
  "v_cvt_pk_bf16_f32 v32, v136, v137\n\t" \
  "v_cvt_pk_bf16_f32 v33, v138, v139\n\t" \
  "s_waitcnt lgkmcnt(0)\n\t" \
  "v_mfma_f32_16x16x32_bf16 v[168:171], v[80:83], v[108:111], v[168:171]\n\t" \
  "ds_read_b64_tr_b16 v[64:65], v116 offset:48\n\t" \
  "ds_read_b64_tr_b16 v[66:67], v116 offset:3632\n\t" \
  "v_mfma_f32_16x16x32_bf16 v[172:175], v[80:83], v[112:115], v[172:175]\n\t" \
  "ds_read_b64_tr_b16 v[68:69], v117 offset:48\n\t" \
  "ds_read_b64_tr_b16 v[70:71], v117 offset:3632\n\t" \
  "s_nop 1\n\t" \
  "v_permlane16_swap_b32 v152, v156\n\t" \
  "v_permlane16_swap_b32 v153, v157\n\t" \
  "v_permlane16_swap_b32 v154, v158\n\t" \
  "v_permlane16_swap_b32 v155, v159\n\t" \
  "v_mfma_f32_16x16x32_bf16 v[172:175], v[84:87], v[60:63], v[172:175]\n\t" \
  "ds_read_b64_tr_b16 v[72:73], v118 offset:48\n\t" \
  "ds_read_b64_tr_b16 v[74:75], v118 offset:3632\n\t" \
  "v_fma_f32 v152, v152, v47, v96\n\t" \
  "v_fma_f32 v153, v153, v47, v97\n\t" \
  "v_fma_f32 v154, v154, v47, v98\n\t" \
  "v_fma_f32 v155, v155, v47, v99\n\t" \
  "v_mfma_f32_16x16x32_bf16 v[168:171], v[84:87], v[56:59], v[168:171]\n\t" \
  "ds_read_b64_tr_b16 v[76:77], v119 offset:48\n\t" \
  "ds_read_b64_tr_b16 v[78:79], v119 offset:3632\n\t" \
  "v_fma_f32 v156, v156, v47, v124\n\t" \
  "v_fma_f32 v157, v157, v47, v125\n\t" \
  "v_fma_f32 v158, v158, v47, v126\n\t" \
  "v_mfma_f32_16x16x32_bf16 v[168:171], v[88:91], v[100:103], v[168:171]\n\t" \
  "v_fma_f32 v159, v159, v47, v127\n\t" \
  "v_maximum3_f32 v152, v152, 0, 0\n\t" \
  "v_maximum3_f32 v153, v153, 0, 0\n\t" \
  "v_mfma_f32_16x16x32_bf16 v[172:175], v[88:91], v[104:107], v[172:175]\n\t" \
  "v_maximum3_f32 v154, v154, 0, 0\n\t" \
  "v_maximum3_f32 v155, v155, 0, 0\n\t" \
  "v_mul_f32 v152, v152, v156\n\t" \
  "v_mfma_f32_16x16x32_bf16 v[172:175], v[92:95], v[244:247], v[172:175]\n\t" \
  "v_mul_f32 v153, v153, v157\n\t" \
  "v_mul_f32 v154, v154, v158\n\t" \
  "v_mul_f32 v155, v155, v159\n\t" \
  "ds_read_b128 v[96:99], v6 offset:64\n\t" \
  "ds_read_b128 v[124:127], v6 offset:512\n\t" \
  "v_mfma_f32_16x16x32_bf16 v[168:171], v[92:95], v[240:243], v[168:171]\n\t" \
  "v_cvt_pk_bf16_f32 v34, v152, v153\n\t" \
  "v_cvt_pk_bf16_f32 v35, v154, v155\n\t" \
  "buffer_store_dwordx4 v[32:35], v7, %[srdA], %[oP1] offen offset:0 sc1\n\t" \
  "s_waitcnt lgkmcnt(0)\n\t" \
  "v_mfma_f32_16x16x32_bf16 v[184:187], v[64:67], v[108:111], v[184:187]\n\t" \
  "ds_read_b64_tr_b16 v[80:81], v116 offset:64\n\t" \
  "ds_read_b64_tr_b16 v[82:83], v116 offset:3648\n\t" \
  "v_mfma_f32_16x16x32_bf16 v[188:191], v[64:67], v[112:115], v[188:191]\n\t" \
  "ds_read_b64_tr_b16 v[84:85], v117 offset:64\n\t" \
  "ds_read_b64_tr_b16 v[86:87], v117 offset:3648\n\t" \
  "s_nop 1\n\t" \
  "v_permlane16_swap_b32 v168, v172\n\t" \
  "v_permlane16_swap_b32 v169, v173\n\t" \
  "v_permlane16_swap_b32 v170, v174\n\t" \
  "v_permlane16_swap_b32 v171, v175\n\t" \
  "v_mfma_f32_16x16x32_bf16 v[188:191], v[68:71], v[60:63], v[188:191]\n\t" \
  "ds_read_b64_tr_b16 v[88:89], v118 offset:64\n\t" \
  "ds_read_b64_tr_b16 v[90:91], v118 offset:3648\n\t" \
  "v_fma_f32 v168, v168, v47, v96\n\t" \
  "v_fma_f32 v169, v169, v47, v97\n\t" \
  "v_fma_f32 v170, v170, v47, v98\n\t" \
  "v_mfma_f32_16x16x32_bf16 v[184:187], v[68:71], v[56:59], v[184:187]\n\t" \
  "ds_read_b64_tr_b16 v[92:93], v119 offset:64\n\t" \
  "ds_read_b64_tr_b16 v[94:95], v119 offset:3648\n\t" \
  "v_fma_f32 v171, v171, v47, v99\n\t" \
  "v_fma_f32 v172, v172, v47, v124\n\t" \
  "v_fma_f32 v173, v173, v47, v125\n\t" \
  "v_mfma_f32_16x16x32_bf16 v[184:187], v[72:75], v[100:103], v[184:187]\n\t" \
  "v_fma_f32 v174, v174, v47, v126\n\t" \
  "v_fma_f32 v175, v175, v47, v127\n\t" \
  "v_maximum3_f32 v168, v168, 0, 0\n\t" \
  "v_mfma_f32_16x16x32_bf16 v[188:191], v[72:75], v[104:107], v[188:191]\n\t" \
  "v_maximum3_f32 v169, v169, 0, 0\n\t" \
  "v_maximum3_f32 v170, v170, 0, 0\n\t" \
  "v_maximum3_f32 v171, v171, 0, 0\n\t" \
  "v_mfma_f32_16x16x32_bf16 v[188:191], v[76:79], v[244:247], v[188:191]\n\t" \
  "v_mul_f32 v168, v168, v172\n\t" \
  "v_mul_f32 v169, v169, v173\n\t" \
  "v_mul_f32 v170, v170, v174\n\t" \
  "ds_read_b128 v[96:99], v6 offset:96\n\t" \
  "ds_read_b128 v[124:127], v6 offset:544\n\t" \
  "v_mfma_f32_16x16x32_bf16 v[184:187], v[76:79], v[240:243], v[184:187]\n\t" \
  "v_mul_f32 v171, v171, v175\n\t" \
  "v_cvt_pk_bf16_f32 v36, v168, v169\n\t" \
  "v_cvt_pk_bf16_f32 v37, v170, v171\n\t" \
  "s_waitcnt lgkmcnt(0)\n\t" \
  "v_mfma_f32_16x16x32_bf16 v[200:203], v[80:83], v[108:111], v[200:203]\n\t" \
  "ds_read_b64_tr_b16 v[64:65], v116 offset:80\n\t" \
  "ds_read_b64_tr_b16 v[66:67], v116 offset:3664\n\t" \
  "v_mfma_f32_16x16x32_bf16 v[204:207], v[80:83], v[112:115], v[204:207]\n\t" \
  "ds_read_b64_tr_b16 v[68:69], v117 offset:80\n\t" \
  "ds_read_b64_tr_b16 v[70:71], v117 offset:3664\n\t" \
  "s_nop 1\n\t" \
  "v_permlane16_swap_b32 v184, v188\n\t" \
  "v_permlane16_swap_b32 v185, v189\n\t" \
  "v_permlane16_swap_b32 v186, v190\n\t" \
  "v_permlane16_swap_b32 v187, v191\n\t" \
  "v_mfma_f32_16x16x32_bf16 v[204:207], v[84:87], v[60:63], v[204:207]\n\t" \
  "ds_read_b64_tr_b16 v[72:73], v118 offset:80\n\t" \
  "ds_read_b64_tr_b16 v[74:75], v118 offset:3664\n\t" \
  "v_fma_f32 v184, v184, v47, v96\n\t" \
  "v_fma_f32 v185, v185, v47, v97\n\t" \
  "v_fma_f32 v186, v186, v47, v98\n\t" \
  "v_fma_f32 v187, v187, v47, v99\n\t" \
  "v_mfma_f32_16x16x32_bf16 v[200:203], v[84:87], v[56:59], v[200:203]\n\t" \
  "ds_read_b64_tr_b16 v[76:77], v119 offset:80\n\t" \
  "ds_read_b64_tr_b16 v[78:79], v119 offset:3664\n\t" \
  "v_fma_f32 v188, v188, v47, v124\n\t" \
  "v_fma_f32 v189, v189, v47, v125\n\t" \
  "v_fma_f32 v190, v190, v47, v126\n\t" \
  "v_mfma_f32_16x16x32_bf16 v[200:203], v[88:91], v[100:103], v[200:203]\n\t" \
  "v_fma_f32 v191, v191, v47, v127\n\t" \
  "v_maximum3_f32 v184, v184, 0, 0\n\t" \
  "v_maximum3_f32 v185, v185, 0, 0\n\t" \
  "v_mfma_f32_16x16x32_bf16 v[204:207], v[88:91], v[104:107], v[204:207]\n\t" \
  "v_maximum3_f32 v186, v186, 0, 0\n\t" \
  "v_maximum3_f32 v187, v187, 0, 0\n\t" \
  "v_mul_f32 v184, v184, v188\n\t" \
  "v_mfma_f32_16x16x32_bf16 v[204:207], v[92:95], v[244:247], v[204:207]\n\t" \
  "v_mul_f32 v185, v185, v189\n\t" \
  "v_mul_f32 v186, v186, v190\n\t" \
  "v_mul_f32 v187, v187, v191\n\t" \
  "ds_read_b128 v[96:99], v6 offset:128\n\t" \
  "ds_read_b128 v[124:127], v6 offset:576\n\t" \
  "v_mfma_f32_16x16x32_bf16 v[200:203], v[92:95], v[240:243], v[200:203]\n\t" \
  "v_cvt_pk_bf16_f32 v38, v184, v185\n\t" \
  "v_cvt_pk_bf16_f32 v39, v186, v187\n\t" \
  "buffer_store_dwordx4 v[36:39], v7, %[srdA], %[oP1] offen offset:512 sc1\n\t" \
  "s_waitcnt lgkmcnt(0)\n\t" \
  "v_mfma_f32_16x16x32_bf16 v[216:219], v[64:67], v[108:111], v[216:219]\n\t" \
  "ds_read_b64_tr_b16 v[80:81], v120 offset:0\n\t" \
  "ds_read_b64_tr_b16 v[82:83], v120 offset:3584\n\t" \
  "v_mfma_f32_16x16x32_bf16 v[220:223], v[64:67], v[112:115], v[220:223]\n\t" \
  "ds_read_b64_tr_b16 v[84:85], v121 offset:0\n\t" \
  "ds_read_b64_tr_b16 v[86:87], v121 offset:3584\n\t" \
  "s_nop 1\n\t" \
  "v_permlane16_swap_b32 v200, v204\n\t" \
  "v_permlane16_swap_b32 v201, v205\n\t" \
  "v_permlane16_swap_b32 v202, v206\n\t" \
  "v_permlane16_swap_b32 v203, v207\n\t" \
  "v_mfma_f32_16x16x32_bf16 v[220:223], v[68:71], v[60:63], v[220:223]\n\t" \
  "ds_read_b64_tr_b16 v[88:89], v122 offset:0\n\t" \
  "ds_read_b64_tr_b16 v[90:91], v122 offset:3584\n\t" \
  "v_fma_f32 v200, v200, v47, v96\n\t" \
  "v_fma_f32 v201, v201, v47, v97\n\t" \
  "v_fma_f32 v202, v202, v47, v98\n\t" \
  "v_mfma_f32_16x16x32_bf16 v[216:219], v[68:71], v[56:59], v[216:219]\n\t" \
  "ds_read_b64_tr_b16 v[92:93], v123 offset:0\n\t" \
  "ds_read_b64_tr_b16 v[94:95], v123 offset:3584\n\t" \
  "v_fma_f32 v203, v203, v47, v99\n\t" \
  "v_fma_f32 v204, v204, v47, v124\n\t" \
  "v_fma_f32 v205, v205, v47, v125\n\t" \
  "v_mfma_f32_16x16x32_bf16 v[216:219], v[72:75], v[100:103], v[216:219]\n\t" \
  "v_fma_f32 v206, v206, v47, v126\n\t" \
  "v_fma_f32 v207, v207, v47, v127\n\t" \
  "v_maximum3_f32 v200, v200, 0, 0\n\t" \
  "v_mfma_f32_16x16x32_bf16 v[220:223], v[72:75], v[104:107], v[220:223]\n\t" \
  "v_maximum3_f32 v201, v201, 0, 0\n\t" \
  "v_maximum3_f32 v202, v202, 0, 0\n\t" \
  "v_maximum3_f32 v203, v203, 0, 0\n\t" \
  "v_mfma_f32_16x16x32_bf16 v[220:223], v[76:79], v[244:247], v[220:223]\n\t" \
  "v_mul_f32 v200, v200, v204\n\t" \
  "v_mul_f32 v201, v201, v205\n\t" \
  "v_mul_f32 v202, v202, v206\n\t" \
  "ds_read_b128 v[96:99], v6 offset:160\n\t" \
  "ds_read_b128 v[124:127], v6 offset:608\n\t" \
  "v_mfma_f32_16x16x32_bf16 v[216:219], v[76:79], v[240:243], v[216:219]\n\t" \
  "v_mul_f32 v203, v203, v207\n\t" \
  "v_cvt_pk_bf16_f32 v40, v200, v201\n\t" \
  "v_cvt_pk_bf16_f32 v41, v202, v203\n\t" \
  "s_waitcnt lgkmcnt(0)\n\t" \
  "v_mfma_f32_16x16x32_bf16 v[232:235], v[80:83], v[108:111], v[232:235]\n\t" \
  "v_mfma_f32_16x16x32_bf16 v[236:239], v[80:83], v[112:115], v[236:239]\n\t" \
  "s_nop 5\n\t" \
  "v_permlane16_swap_b32 v216, v220\n\t" \
  "v_permlane16_swap_b32 v217, v221\n\t" \
  "v_permlane16_swap_b32 v218, v222\n\t" \
  "v_permlane16_swap_b32 v219, v223\n\t" \
  "v_mfma_f32_16x16x32_bf16 v[236:239], v[84:87], v[60:63], v[236:239]\n\t" \
  "v_fma_f32 v216, v216, v47, v96\n\t" \
  "v_fma_f32 v217, v217, v47, v97\n\t" \
  "v_fma_f32 v218, v218, v47, v98\n\t" \
  "v_fma_f32 v219, v219, v47, v99\n\t" \
  "v_mfma_f32_16x16x32_bf16 v[232:235], v[84:87], v[56:59], v[232:235]\n\t" \
  "v_fma_f32 v220, v220, v47, v124\n\t" \
  "v_fma_f32 v221, v221, v47, v125\n\t" \
  "v_fma_f32 v222, v222, v47, v126\n\t" \
  "v_mfma_f32_16x16x32_bf16 v[232:235], v[88:91], v[100:103], v[232:235]\n\t" \
  "v_fma_f32 v223, v223, v47, v127\n\t" \
  "v_maximum3_f32 v216, v216, 0, 0\n\t" \
  "v_maximum3_f32 v217, v217, 0, 0\n\t" \
  "v_mfma_f32_16x16x32_bf16 v[236:239], v[88:91], v[104:107], v[236:239]\n\t" \
  "v_maximum3_f32 v218, v218, 0, 0\n\t" \
  "v_maximum3_f32 v219, v219, 0, 0\n\t" \
  "v_mul_f32 v216, v216, v220\n\t" \
  "v_mfma_f32_16x16x32_bf16 v[236:239], v[92:95], v[244:247], v[236:239]\n\t" \
  "v_mul_f32 v217, v217, v221\n\t" \
  "v_mul_f32 v218, v218, v222\n\t" \
  "v_mul_f32 v219, v219, v223\n\t" \
  "ds_read_b128 v[96:99], v14 offset:0\n\t" \
  "ds_read_b128 v[124:127], v14 offset:448\n\t" \
  "v_mfma_f32_16x16x32_bf16 v[232:235], v[92:95], v[240:243], v[232:235]\n\t" \
  "v_cvt_pk_bf16_f32 v42, v216, v217\n\t" \
  "v_cvt_pk_bf16_f32 v43, v218, v219\n\t" \
  "buffer_store_dwordx4 v[40:43], v7, %[srdA], %[oP1] offen offset:1024 sc1\n\t" \
  "s_waitcnt lgkmcnt(0)\n\t" \
  "s_nop 7\n\t" \
  "v_permlane16_swap_b32 v232, v236\n\t" \
  "v_permlane16_swap_b32 v233, v237\n\t" \
  "v_permlane16_swap_b32 v234, v238\n\t" \
  "v_permlane16_swap_b32 v235, v239\n\t" \
  "v_fma_f32 v232, v232, v47, v96\n\t" \
  "v_fma_f32 v233, v233, v47, v97\n\t" \
  "v_fma_f32 v234, v234, v47, v98\n\t" \
  "v_fma_f32 v235, v235, v47, v99\n\t" \
  "v_fma_f32 v236, v236, v47, v124\n\t" \
  "v_fma_f32 v237, v237, v47, v125\n\t" \
  "v_fma_f32 v238, v238, v47, v126\n\t" \
  "v_fma_f32 v239, v239, v47, v127\n\t" \
  "v_maximum3_f32 v232, v232, 0, 0\n\t" \
  "v_maximum3_f32 v233, v233, 0, 0\n\t" \
  "v_maximum3_f32 v234, v234, 0, 0\n\t" \
  "v_maximum3_f32 v235, v235, 0, 0\n\t" \
  "v_mul_f32 v232, v232, v236\n\t" \
  "v_mul_f32 v233, v233, v237\n\t" \
  "v_mul_f32 v234, v234, v238\n\t" \
  "v_mul_f32 v235, v235, v239\n\t" \
  "v_cvt_pk_bf16_f32 v44, v232, v233\n\t" \
  "v_cvt_pk_bf16_f32 v45, v234, v235\n\t" \
  "buffer_store_dwordx2 v[44:45], v7, %[srdA], %[oL1] offen sc1\n\t" \
  "s_nop 1\n\t" \
  ""
#define SG_ASM_LOOP \
  "buffer_load_dwordx4 v[96:99], %[vt0], %[srdT], %[sT] offen offset:0 sc1\n\t" \
  "buffer_load_dwordx4 v[100:103], %[vt1], %[srdT], %[sT] offen offset:0 sc1\n\t" \
  "buffer_load_dwordx4 v[104:107], %[vt0], %[srdT], %[sT] offen offset:2048 sc1\n\t" \
  "buffer_load_dwordx4 v[108:111], %[vt1], %[srdT], %[sT] offen offset:2048 sc1\n\t" \
  "s_add_u32 %[sT], %[sT], 0x1000\n\t" \
  "s_barrier\n\t" \
  "buffer_load_dwordx4 v[112:115], %[vt0], %[srdT], %[sT] offen offset:0 sc1\n\t" \
  "buffer_load_dwordx4 v[116:119], %[vt1], %[srdT], %[sT] offen offset:0 sc1\n\t" \
  "buffer_load_dwordx4 v[120:123], %[vt0], %[srdT], %[sT] offen offset:2048 sc1\n\t" \
  "buffer_load_dwordx4 v[124:127], %[vt1], %[srdT], %[sT] offen offset:2048 sc1\n\t" \
  "s_add_u32 %[sT], %[sT], 0x1000\n\t" \
  "s_barrier\n\t" \
  "buffer_load_dwordx4 v[128:131], %[vt0], %[srdT], %[sT] offen offset:0 sc1\n\t" \
  "buffer_load_dwordx4 v[132:135], %[vt1], %[srdT], %[sT] offen offset:0 sc1\n\t" \
  "buffer_load_dwordx4 v[136:139], %[vt0], %[srdT], %[sT] offen offset:2048 sc1\n\t" \
  "buffer_load_dwordx4 v[140:143], %[vt1], %[srdT], %[sT] offen offset:2048 sc1\n\t" \
  "s_add_u32 %[sT], %[sT], 0x1000\n\t" \
  "buffer_load_dwordx4 v[144:147], %[vt0], %[srdT], %[sT] offen offset:0 sc1\n\t" \
  "buffer_load_dwordx4 v[148:151], %[vt1], %[srdT], %[sT] offen offset:0 sc1\n\t" \
  "buffer_load_dwordx4 v[152:155], %[vt0], %[srdT], %[sT] offen offset:2048 sc1\n\t" \
  "buffer_load_dwordx4 v[156:159], %[vt1], %[srdT], %[sT] offen offset:2048 sc1\n\t" \
  "s_add_u32 %[sT], %[sT], 0x1000\n\t" \
  "v_mov_b64 v[32:33], 0\n\t" \
  "v_mov_b64 v[34:35], 0\n\t" \
  "v_mov_b64 v[36:37], 0\n\t" \
  "v_mov_b64 v[38:39], 0\n\t" \
  "v_mov_b64 v[40:41], 0\n\t" \
  "v_mov_b64 v[42:43], 0\n\t" \
  "v_mov_b64 v[44:45], 0\n\t" \
  "v_mov_b64 v[46:47], 0\n\t" \
  "v_mov_b64 v[48:49], 0\n\t" \
  "v_mov_b64 v[50:51], 0\n\t" \
  "v_mov_b64 v[52:53], 0\n\t" \
  "v_mov_b64 v[54:55], 0\n\t" \
  "v_mov_b64 v[56:57], 0\n\t" \
  "v_mov_b64 v[58:59], 0\n\t" \
  "v_mov_b64 v[60:61], 0\n\t" \
  "v_mov_b64 v[62:63], 0\n\t" \
  "v_mov_b32 v64, %[tl]\n\t" \
  "v_mov_b32 v65, %[th]\n\t" \
  "s_waitcnt vmcnt(16)\n\t" \
  "s_cmp_lg_u32 %[ld], 0\n\t" \
  "s_cbranch_scc1 1f\n\t" \
  "buffer_store_dwordx2 v[64:65], off, %[srdX], %[oK] sc1\n\t" \
  "1:\n\t" \
  "s_barrier\n\t" \
  "ds_read_b64_tr_b16 v[64:65], %[a0] offset:0\n\t" \
  "ds_read_b64_tr_b16 v[66:67], %[a0] offset:2048\n\t" \
  "ds_read_b64_tr_b16 v[68:69], %[a1] offset:0\n\t" \
  "ds_read_b64_tr_b16 v[70:71], %[a1] offset:2048\n\t" \
  "ds_read_b64_tr_b16 v[72:73], %[a2] offset:0\n\t" \
  "ds_read_b64_tr_b16 v[74:75], %[a2] offset:2048\n\t" \
  "ds_read_b64_tr_b16 v[76:77], %[a3] offset:0\n\t" \
  "ds_read_b64_tr_b16 v[78:79], %[a3] offset:2048\n\t" \
  "ds_read_b64_tr_b16 v[80:81], %[a0] offset:8192\n\t" \
  "ds_read_b64_tr_b16 v[82:83], %[a0] offset:10240\n\t" \
  "ds_read_b64_tr_b16 v[84:85], %[a1] offset:8192\n\t" \
  "ds_read_b64_tr_b16 v[86:87], %[a1] offset:10240\n\t" \
  "ds_read_b64_tr_b16 v[88:89], %[a2] offset:8192\n\t" \
  "ds_read_b64_tr_b16 v[90:91], %[a2] offset:10240\n\t" \
  "ds_read_b64_tr_b16 v[92:93], %[a3] offset:8192\n\t" \
  "ds_read_b64_tr_b16 v[94:95], %[a3] offset:10240\n\t" \
  "s_waitcnt vmcnt(12)\n\t" \
  "s_waitcnt lgkmcnt(14)\n\t" \
  "v_mfma_f32_16x16x32_bf16 v[32:35], v[64:67], v[96:99], v[32:35]\n\t" \
  "s_add_u32 m0, %[dW], 131072\n\t" \
  "v_mfma_f32_16x16x32_bf16 v[36:39], v[64:67], v[100:103], v[36:39]\n\t" \
  "ds_read_b64_tr_b16 v[64:65], %[a0] offset:16384\n\t" \
  "ds_read_b64_tr_b16 v[66:67], %[a0] offset:18432\n\t" \
  "s_waitcnt lgkmcnt(14)\n\t" \
  "v_mfma_f32_16x16x32_bf16 v[44:47], v[68:71], v[100:103], v[44:47]\n\t" \
  "buffer_load_dwordx4 %[vW], %[srdW], %[sW] offen lds\n\t" \
  "v_mfma_f32_16x16x32_bf16 v[40:43], v[68:71], v[96:99], v[40:43]\n\t" \
  "ds_read_b64_tr_b16 v[68:69], %[a1] offset:16384\n\t" \
  "ds_read_b64_tr_b16 v[70:71], %[a1] offset:18432\n\t" \
  "s_waitcnt lgkmcnt(14)\n\t" \
  "v_mfma_f32_16x16x32_bf16 v[48:51], v[72:75], v[96:99], v[48:51]\n\t" \
  "v_mfma_f32_16x16x32_bf16 v[52:55], v[72:75], v[100:103], v[52:55]\n\t" \
  "ds_read_b64_tr_b16 v[72:73], %[a2] offset:16384\n\t" \
  "ds_read_b64_tr_b16 v[74:75], %[a2] offset:18432\n\t" \
  "s_waitcnt lgkmcnt(14)\n\t" \
  "v_mfma_f32_16x16x32_bf16 v[60:63], v[76:79], v[100:103], v[60:63]\n\t" \
  "buffer_load_dwordx4 v[100:103], %[vt1], %[srdT], %[sT] offen offset:0 sc1\n\t" \
  "v_mfma_f32_16x16x32_bf16 v[56:59], v[76:79], v[96:99], v[56:59]\n\t" \
  "ds_read_b64_tr_b16 v[76:77], %[a3] offset:16384\n\t" \
  "ds_read_b64_tr_b16 v[78:79], %[a3] offset:18432\n\t" \
  "buffer_load_dwordx4 v[96:99], %[vt0], %[srdT], %[sT] offen offset:0 sc1\n\t" \
  "s_waitcnt lgkmcnt(14)\n\t" \
  "v_mfma_f32_16x16x32_bf16 v[32:35], v[80:83], v[104:107], v[32:35]\n\t" \
  "s_add_u32 m0, %[dW], 132096\n\t" \
  "v_mfma_f32_16x16x32_bf16 v[36:39], v[80:83], v[108:111], v[36:39]\n\t" \
  "ds_read_b64_tr_b16 v[80:81], %[a0] offset:24576\n\t" \
  "ds_read_b64_tr_b16 v[82:83], %[a0] offset:26624\n\t" \
  "s_waitcnt lgkmcnt(14)\n\t" \
  "v_mfma_f32_16x16x32_bf16 v[44:47], v[84:87], v[108:111], v[44:47]\n\t" \
  "buffer_load_dwordx4 %[vW2], %[srdW], %[sW] offen lds\n\t" \
  "v_mfma_f32_16x16x32_bf16 v[40:43], v[84:87], v[104:107], v[40:43]\n\t" \
  "ds_read_b64_tr_b16 v[84:85], %[a1] offset:24576\n\t" \
  "ds_read_b64_tr_b16 v[86:87], %[a1] offset:26624\n\t" \
  "s_waitcnt lgkmcnt(14)\n\t" \
  "v_mfma_f32_16x16x32_bf16 v[48:51], v[88:91], v[104:107], v[48:51]\n\t" \
  "s_add_u32 %[sW], %[sW], 262144\n\t" \
  "v_mfma_f32_16x16x32_bf16 v[52:55], v[88:91], v[108:111], v[52:55]\n\t" \
  "ds_read_b64_tr_b16 v[88:89], %[a2] offset:24576\n\t" \
  "ds_read_b64_tr_b16 v[90:91], %[a2] offset:26624\n\t" \
  "s_waitcnt lgkmcnt(14)\n\t" \
  "v_mfma_f32_16x16x32_bf16 v[60:63], v[92:95], v[108:111], v[60:63]\n\t" \
  "buffer_load_dwordx4 v[108:111], %[vt1], %[srdT], %[sT] offen offset:2048 sc1\n\t" \
  "v_mfma_f32_16x16x32_bf16 v[56:59], v[92:95], v[104:107], v[56:59]\n\t" \
  "ds_read_b64_tr_b16 v[92:93], %[a3] offset:24576\n\t" \
  "ds_read_b64_tr_b16 v[94:95], %[a3] offset:26624\n\t" \
  "buffer_load_dwordx4 v[104:107], %[vt0], %[srdT], %[sT] offen offset:2048 sc1\n\t" \
  "s_add_u32 %[sT], %[sT], 0x1000\n\t" \
  "s_waitcnt vmcnt(14)\n\t" \
  "s_waitcnt lgkmcnt(14)\n\t" \
  "v_mfma_f32_16x16x32_bf16 v[32:35], v[64:67], v[112:115], v[32:35]\n\t" \
  "s_add_u32 m0, %[dW], 147456\n\t" \
  "v_mfma_f32_16x16x32_bf16 v[36:39], v[64:67], v[116:119], v[36:39]\n\t" \
  "ds_read_b64_tr_b16 v[64:65], %[a0] offset:32768\n\t" \
  "ds_read_b64_tr_b16 v[66:67], %[a0] offset:34816\n\t" \
  "s_waitcnt lgkmcnt(14)\n\t" \
  "v_mfma_f32_16x16x32_bf16 v[44:47], v[68:71], v[116:119], v[44:47]\n\t" \
  "buffer_load_dwordx4 %[vW], %[srdW], %[sW] offen lds\n\t" \
  "v_mfma_f32_16x16x32_bf16 v[40:43], v[68:71], v[112:115], v[40:43]\n\t" \
  "ds_read_b64_tr_b16 v[68:69], %[a1] offset:32768\n\t" \
  "ds_read_b64_tr_b16 v[70:71], %[a1] offset:34816\n\t" \
  "s_waitcnt lgkmcnt(14)\n\t" \
  "v_mfma_f32_16x16x32_bf16 v[48:51], v[72:75], v[112:115], v[48:51]\n\t" \
  "v_mfma_f32_16x16x32_bf16 v[52:55], v[72:75], v[116:119], v[52:55]\n\t" \
  "ds_read_b64_tr_b16 v[72:73], %[a2] offset:32768\n\t" \
  "ds_read_b64_tr_b16 v[74:75], %[a2] offset:34816\n\t" \
  "s_waitcnt lgkmcnt(14)\n\t" \
  "v_mfma_f32_16x16x32_bf16 v[60:63], v[76:79], v[116:119], v[60:63]\n\t" \
  "buffer_load_dwordx4 v[116:119], %[vt1], %[srdT], %[sT] offen offset:0 sc1\n\t" \
  "v_mfma_f32_16x16x32_bf16 v[56:59], v[76:79], v[112:115], v[56:59]\n\t" \
  "ds_read_b64_tr_b16 v[76:77], %[a3] offset:32768\n\t" \
  "ds_read_b64_tr_b16 v[78:79], %[a3] offset:34816\n\t" \
  "buffer_load_dwordx4 v[112:115], %[vt0], %[srdT], %[sT] offen offset:0 sc1\n\t" \
  "s_waitcnt lgkmcnt(14)\n\t" \
  "v_mfma_f32_16x16x32_bf16 v[32:35], v[80:83], v[120:123], v[32:35]\n\t" \
  "s_add_u32 m0, %[dW], 148480\n\t" \
  "v_mfma_f32_16x16x32_bf16 v[36:39], v[80:83], v[124:127], v[36:39]\n\t" \
  "ds_read_b64_tr_b16 v[80:81], %[a0] offset:40960\n\t" \
  "ds_read_b64_tr_b16 v[82:83], %[a0] offset:43008\n\t" \
  "s_waitcnt lgkmcnt(14)\n\t" \
  "v_mfma_f32_16x16x32_bf16 v[44:47], v[84:87], v[124:127], v[44:47]\n\t" \
  "buffer_load_dwordx4 %[vW2], %[srdW], %[sW] offen lds\n\t" \
  "v_mfma_f32_16x16x32_bf16 v[40:43], v[84:87], v[120:123], v[40:43]\n\t" \
  "ds_read_b64_tr_b16 v[84:85], %[a1] offset:40960\n\t" \
  "ds_read_b64_tr_b16 v[86:87], %[a1] offset:43008\n\t" \
  "s_waitcnt lgkmcnt(14)\n\t" \
  "v_mfma_f32_16x16x32_bf16 v[48:51], v[88:91], v[120:123], v[48:51]\n\t" \
  "s_add_u32 %[sW], %[sW], 262144\n\t" \
  "v_mfma_f32_16x16x32_bf16 v[52:55], v[88:91], v[124:127], v[52:55]\n\t" \
  "ds_read_b64_tr_b16 v[88:89], %[a2] offset:40960\n\t" \
  "ds_read_b64_tr_b16 v[90:91], %[a2] offset:43008\n\t" \
  "s_waitcnt lgkmcnt(14)\n\t" \
  "v_mfma_f32_16x16x32_bf16 v[60:63], v[92:95], v[124:127], v[60:63]\n\t" \
  "buffer_load_dwordx4 v[124:127], %[vt1], %[srdT], %[sT] offen offset:2048 sc1\n\t" \
  "v_mfma_f32_16x16x32_bf16 v[56:59], v[92:95], v[120:123], v[56:59]\n\t" \
  "ds_read_b64_tr_b16 v[92:93], %[a3] offset:40960\n\t" \
  "ds_read_b64_tr_b16 v[94:95], %[a3] offset:43008\n\t" \
  "buffer_load_dwordx4 v[120:123], %[vt0], %[srdT], %[sT] offen offset:2048 sc1\n\t" \
  "s_add_u32 %[sT], %[sT], 0x1000\n\t" \
  "s_waitcnt vmcnt(16)\n\t" \
  "s_waitcnt lgkmcnt(14)\n\t" \
  "v_mfma_f32_16x16x32_bf16 v[32:35], v[64:67], v[128:131], v[32:35]\n\t" \
  "v_mfma_f32_16x16x32_bf16 v[36:39], v[64:67], v[132:135], v[36:39]\n\t" \
  "ds_read_b64_tr_b16 v[64:65], %[a0] offset:49152\n\t" \
  "ds_read_b64_tr_b16 v[66:67], %[a0] offset:51200\n\t" \
  "s_waitcnt lgkmcnt(14)\n\t" \
  "v_mfma_f32_16x16x32_bf16 v[44:47], v[68:71], v[132:135], v[44:47]\n\t" \
  "v_mfma_f32_16x16x32_bf16 v[40:43], v[68:71], v[128:131], v[40:43]\n\t" \
  "ds_read_b64_tr_b16 v[68:69], %[a1] offset:49152\n\t" \
  "ds_read_b64_tr_b16 v[70:71], %[a1] offset:51200\n\t" \
  "s_waitcnt lgkmcnt(14)\n\t" \
  "v_mfma_f32_16x16x32_bf16 v[48:51], v[72:75], v[128:131], v[48:51]\n\t" \
  "v_mfma_f32_16x16x32_bf16 v[52:55], v[72:75], v[132:135], v[52:55]\n\t" \
  "ds_read_b64_tr_b16 v[72:73], %[a2] offset:49152\n\t" \
  "ds_read_b64_tr_b16 v[74:75], %[a2] offset:51200\n\t" \
  "s_waitcnt lgkmcnt(14)\n\t" \
  "v_mfma_f32_16x16x32_bf16 v[60:63], v[76:79], v[132:135], v[60:63]\n\t" \
  "buffer_load_dwordx4 v[132:135], %[vt1], %[srdT], %[sT] offen offset:0 sc1\n\t" \
  "v_mfma_f32_16x16x32_bf16 v[56:59], v[76:79], v[128:131], v[56:59]\n\t" \
  "ds_read_b64_tr_b16 v[76:77], %[a3] offset:49152\n\t" \
  "ds_read_b64_tr_b16 v[78:79], %[a3] offset:51200\n\t" \
  "buffer_load_dwordx4 v[128:131], %[vt0], %[srdT], %[sT] offen offset:0 sc1\n\t" \
  "s_waitcnt lgkmcnt(14)\n\t" \
  "v_mfma_f32_16x16x32_bf16 v[32:35], v[80:83], v[136:139], v[32:35]\n\t" \
  "v_mfma_f32_16x16x32_bf16 v[36:39], v[80:83], v[140:143], v[36:39]\n\t" \
  "ds_read_b64_tr_b16 v[80:81], %[a0] offset:57344\n\t" \
  "ds_read_b64_tr_b16 v[82:83], %[a0] offset:59392\n\t" \
  "s_waitcnt lgkmcnt(14)\n\t" \
  "v_mfma_f32_16x16x32_bf16 v[44:47], v[84:87], v[140:143], v[44:47]\n\t" \
  "v_mfma_f32_16x16x32_bf16 v[40:43], v[84:87], v[136:139], v[40:43]\n\t" \
  "ds_read_b64_tr_b16 v[84:85], %[a1] offset:57344\n\t" \
  "ds_read_b64_tr_b16 v[86:87], %[a1] offset:59392\n\t" \
  "s_waitcnt lgkmcnt(14)\n\t" \
  "v_mfma_f32_16x16x32_bf16 v[48:51], v[88:91], v[136:139], v[48:51]\n\t" \
  "v_mfma_f32_16x16x32_bf16 v[52:55], v[88:91], v[140:143], v[52:55]\n\t" \
  "ds_read_b64_tr_b16 v[88:89], %[a2] offset:57344\n\t" \
  "ds_read_b64_tr_b16 v[90:91], %[a2] offset:59392\n\t" \
  "s_waitcnt lgkmcnt(14)\n\t" \
  "v_mfma_f32_16x16x32_bf16 v[60:63], v[92:95], v[140:143], v[60:63]\n\t" \
  "buffer_load_dwordx4 v[140:143], %[vt1], %[srdT], %[sT] offen offset:2048 sc1\n\t" \
  "v_mfma_f32_16x16x32_bf16 v[56:59], v[92:95], v[136:139], v[56:59]\n\t" \
  "ds_read_b64_tr_b16 v[92:93], %[a3] offset:57344\n\t" \
  "ds_read_b64_tr_b16 v[94:95], %[a3] offset:59392\n\t" \
  "buffer_load_dwordx4 v[136:139], %[vt0], %[srdT], %[sT] offen offset:2048 sc1\n\t" \
  "s_add_u32 %[sT], %[sT], 0x1000\n\t" \
  "s_waitcnt vmcnt(16)\n\t" \
  "s_waitcnt lgkmcnt(14)\n\t" \
  "v_mfma_f32_16x16x32_bf16 v[32:35], v[64:67], v[144:147], v[32:35]\n\t" \
  "v_mfma_f32_16x16x32_bf16 v[36:39], v[64:67], v[148:151], v[36:39]\n\t" \
  "ds_read_b64_tr_b16 v[64:65], %[b0] offset:0\n\t" \
  "ds_read_b64_tr_b16 v[66:67], %[b0] offset:2048\n\t" \
  "s_waitcnt lgkmcnt(14)\n\t" \
  "v_mfma_f32_16x16x32_bf16 v[44:47], v[68:71], v[148:151], v[44:47]\n\t" \
  "v_mfma_f32_16x16x32_bf16 v[40:43], v[68:71], v[144:147], v[40:43]\n\t" \
  "ds_read_b64_tr_b16 v[68:69], %[b1] offset:0\n\t" \
  "ds_read_b64_tr_b16 v[70:71], %[b1] offset:2048\n\t" \
  "s_waitcnt lgkmcnt(14)\n\t" \
  "v_mfma_f32_16x16x32_bf16 v[48:51], v[72:75], v[144:147], v[48:51]\n\t" \
  "v_mfma_f32_16x16x32_bf16 v[52:55], v[72:75], v[148:151], v[52:55]\n\t" \
  "ds_read_b64_tr_b16 v[72:73], %[b2] offset:0\n\t" \
  "ds_read_b64_tr_b16 v[74:75], %[b2] offset:2048\n\t" \
  "s_waitcnt lgkmcnt(14)\n\t" \
  "v_mfma_f32_16x16x32_bf16 v[60:63], v[76:79], v[148:151], v[60:63]\n\t" \
  "buffer_load_dwordx4 v[148:151], %[vt1], %[srdT], %[sT] offen offset:0 sc1\n\t" \
  "v_mfma_f32_16x16x32_bf16 v[56:59], v[76:79], v[144:147], v[56:59]\n\t" \
  "ds_read_b64_tr_b16 v[76:77], %[b3] offset:0\n\t" \
  "ds_read_b64_tr_b16 v[78:79], %[b3] offset:2048\n\t" \
  "buffer_load_dwordx4 v[144:147], %[vt0], %[srdT], %[sT] offen offset:0 sc1\n\t" \
  "s_waitcnt lgkmcnt(14)\n\t" \
  "v_mfma_f32_16x16x32_bf16 v[32:35], v[80:83], v[152:155], v[32:35]\n\t" \
  "v_mfma_f32_16x16x32_bf16 v[36:39], v[80:83], v[156:159], v[36:39]\n\t" \
  "ds_read_b64_tr_b16 v[80:81], %[b0] offset:8192\n\t" \
  "ds_read_b64_tr_b16 v[82:83], %[b0] offset:10240\n\t" \
  "s_waitcnt lgkmcnt(14)\n\t" \
  "v_mfma_f32_16x16x32_bf16 v[44:47], v[84:87], v[156:159], v[44:47]\n\t" \
  "v_mfma_f32_16x16x32_bf16 v[40:43], v[84:87], v[152:155], v[40:43]\n\t" \
  "ds_read_b64_tr_b16 v[84:85], %[b1] offset:8192\n\t" \
  "ds_read_b64_tr_b16 v[86:87], %[b1] offset:10240\n\t" \
  "s_waitcnt lgkmcnt(14)\n\t" \
  "v_mfma_f32_16x16x32_bf16 v[48:51], v[88:91], v[152:155], v[48:51]\n\t" \
  "v_mfma_f32_16x16x32_bf16 v[52:55], v[88:91], v[156:159], v[52:55]\n\t" \
  "ds_read_b64_tr_b16 v[88:89], %[b2] offset:8192\n\t" \
  "ds_read_b64_tr_b16 v[90:91], %[b2] offset:10240\n\t" \
  "s_waitcnt lgkmcnt(14)\n\t" \
  "v_mfma_f32_16x16x32_bf16 v[60:63], v[92:95], v[156:159], v[60:63]\n\t" \
  "buffer_load_dwordx4 v[156:159], %[vt1], %[srdT], %[sT] offen offset:2048 sc1\n\t" \
  "v_mfma_f32_16x16x32_bf16 v[56:59], v[92:95], v[152:155], v[56:59]\n\t" \
  "ds_read_b64_tr_b16 v[92:93], %[b3] offset:8192\n\t" \
  "ds_read_b64_tr_b16 v[94:95], %[b3] offset:10240\n\t" \
  "buffer_load_dwordx4 v[152:155], %[vt0], %[srdT], %[sT] offen offset:2048 sc1\n\t" \
  "s_add_u32 %[sT], %[sT], 0x1000\n\t" \
  "s_waitcnt vmcnt(14)\n\t" \
  "s_waitcnt lgkmcnt(14)\n\t" \
  "v_mfma_f32_16x16x32_bf16 v[32:35], v[64:67], v[96:99], v[32:35]\n\t" \
  "v_mfma_f32_16x16x32_bf16 v[36:39], v[64:67], v[100:103], v[36:39]\n\t" \
  "ds_read_b64_tr_b16 v[64:65], %[b0] offset:16384\n\t" \
  "ds_read_b64_tr_b16 v[66:67], %[b0] offset:18432\n\t" \
  "s_waitcnt lgkmcnt(14)\n\t" \
  "v_mfma_f32_16x16x32_bf16 v[44:47], v[68:71], v[100:103], v[44:47]\n\t" \
  "v_mfma_f32_16x16x32_bf16 v[40:43], v[68:71], v[96:99], v[40:43]\n\t" \
  "ds_read_b64_tr_b16 v[68:69], %[b1] offset:16384\n\t" \
  "ds_read_b64_tr_b16 v[70:71], %[b1] offset:18432\n\t" \
  "s_waitcnt lgkmcnt(14)\n\t" \
  "v_mfma_f32_16x16x32_bf16 v[48:51], v[72:75], v[96:99], v[48:51]\n\t" \
  "v_mfma_f32_16x16x32_bf16 v[52:55], v[72:75], v[100:103], v[52:55]\n\t" \
  "ds_read_b64_tr_b16 v[72:73], %[b2] offset:16384\n\t" \
  "ds_read_b64_tr_b16 v[74:75], %[b2] offset:18432\n\t" \
  "s_waitcnt lgkmcnt(14)\n\t" \
  "v_mfma_f32_16x16x32_bf16 v[60:63], v[76:79], v[100:103], v[60:63]\n\t" \
  "v_mfma_f32_16x16x32_bf16 v[56:59], v[76:79], v[96:99], v[56:59]\n\t" \
  "ds_read_b64_tr_b16 v[76:77], %[b3] offset:16384\n\t" \
  "ds_read_b64_tr_b16 v[78:79], %[b3] offset:18432\n\t" \
  "s_waitcnt lgkmcnt(14)\n\t" \
  "v_mfma_f32_16x16x32_bf16 v[32:35], v[80:83], v[104:107], v[32:35]\n\t" \
  "v_mfma_f32_16x16x32_bf16 v[36:39], v[80:83], v[108:111], v[36:39]\n\t" \
  "ds_read_b64_tr_b16 v[80:81], %[b0] offset:24576\n\t" \
  "ds_read_b64_tr_b16 v[82:83], %[b0] offset:26624\n\t" \
  "s_waitcnt lgkmcnt(14)\n\t" \
  "v_mfma_f32_16x16x32_bf16 v[44:47], v[84:87], v[108:111], v[44:47]\n\t" \
  "v_mfma_f32_16x16x32_bf16 v[40:43], v[84:87], v[104:107], v[40:43]\n\t" \
  "ds_read_b64_tr_b16 v[84:85], %[b1] offset:24576\n\t" \
  "ds_read_b64_tr_b16 v[86:87], %[b1] offset:26624\n\t" \
  "s_waitcnt lgkmcnt(14)\n\t" \
  "v_mfma_f32_16x16x32_bf16 v[48:51], v[88:91], v[104:107], v[48:51]\n\t" \
  "v_mfma_f32_16x16x32_bf16 v[52:55], v[88:91], v[108:111], v[52:55]\n\t" \
  "ds_read_b64_tr_b16 v[88:89], %[b2] offset:24576\n\t" \
  "ds_read_b64_tr_b16 v[90:91], %[b2] offset:26624\n\t" \
  "s_waitcnt lgkmcnt(14)\n\t" \
  "v_mfma_f32_16x16x32_bf16 v[60:63], v[92:95], v[108:111], v[60:63]\n\t" \
  "v_mfma_f32_16x16x32_bf16 v[56:59], v[92:95], v[104:107], v[56:59]\n\t" \
  "ds_read_b64_tr_b16 v[92:93], %[b3] offset:24576\n\t" \
  "ds_read_b64_tr_b16 v[94:95], %[b3] offset:26624\n\t" \
  "s_waitcnt vmcnt(8)\n\t" \
  "s_barrier\n\t" \
  "s_waitcnt lgkmcnt(14)\n\t" \
  "v_mfma_f32_16x16x32_bf16 v[32:35], v[64:67], v[112:115], v[32:35]\n\t" \
  "v_mfma_f32_16x16x32_bf16 v[36:39], v[64:67], v[116:119], v[36:39]\n\t" \
  "ds_read_b64_tr_b16 v[64:65], %[b0] offset:32768\n\t" \
  "ds_read_b64_tr_b16 v[66:67], %[b0] offset:34816\n\t" \
  "s_waitcnt lgkmcnt(14)\n\t" \
  "v_mfma_f32_16x16x32_bf16 v[44:47], v[68:71], v[116:119], v[44:47]\n\t" \
  "v_mfma_f32_16x16x32_bf16 v[40:43], v[68:71], v[112:115], v[40:43]\n\t" \
  "ds_read_b64_tr_b16 v[68:69], %[b1] offset:32768\n\t" \
  "ds_read_b64_tr_b16 v[70:71], %[b1] offset:34816\n\t" \
  "s_waitcnt lgkmcnt(14)\n\t" \
  "v_mfma_f32_16x16x32_bf16 v[48:51], v[72:75], v[112:115], v[48:51]\n\t" \
  "v_mfma_f32_16x16x32_bf16 v[52:55], v[72:75], v[116:119], v[52:55]\n\t" \
  "ds_read_b64_tr_b16 v[72:73], %[b2] offset:32768\n\t" \
  "ds_read_b64_tr_b16 v[74:75], %[b2] offset:34816\n\t" \
  "s_waitcnt lgkmcnt(14)\n\t" \
  "v_mfma_f32_16x16x32_bf16 v[60:63], v[76:79], v[116:119], v[60:63]\n\t" \
  "v_mfma_f32_16x16x32_bf16 v[56:59], v[76:79], v[112:115], v[56:59]\n\t" \
  "ds_read_b64_tr_b16 v[76:77], %[b3] offset:32768\n\t" \
  "ds_read_b64_tr_b16 v[78:79], %[b3] offset:34816\n\t" \
  "s_waitcnt lgkmcnt(14)\n\t" \
  "v_mfma_f32_16x16x32_bf16 v[32:35], v[80:83], v[120:123], v[32:35]\n\t" \
  "v_mfma_f32_16x16x32_bf16 v[36:39], v[80:83], v[124:127], v[36:39]\n\t" \
  "ds_read_b64_tr_b16 v[80:81], %[b0] offset:40960\n\t" \
  "ds_read_b64_tr_b16 v[82:83], %[b0] offset:43008\n\t" \
  "s_waitcnt lgkmcnt(14)\n\t" \
  "v_mfma_f32_16x16x32_bf16 v[44:47], v[84:87], v[124:127], v[44:47]\n\t" \
  "v_mfma_f32_16x16x32_bf16 v[40:43], v[84:87], v[120:123], v[40:43]\n\t" \
  "ds_read_b64_tr_b16 v[84:85], %[b1] offset:40960\n\t" \
  "ds_read_b64_tr_b16 v[86:87], %[b1] offset:43008\n\t" \
  "s_waitcnt lgkmcnt(14)\n\t" \
  "v_mfma_f32_16x16x32_bf16 v[48:51], v[88:91], v[120:123], v[48:51]\n\t" \
  "v_mfma_f32_16x16x32_bf16 v[52:55], v[88:91], v[124:127], v[52:55]\n\t" \
  "ds_read_b64_tr_b16 v[88:89], %[b2] offset:40960\n\t" \
  "ds_read_b64_tr_b16 v[90:91], %[b2] offset:43008\n\t" \
  "s_waitcnt lgkmcnt(14)\n\t" \
  "v_mfma_f32_16x16x32_bf16 v[60:63], v[92:95], v[124:127], v[60:63]\n\t" \
  "v_mfma_f32_16x16x32_bf16 v[56:59], v[92:95], v[120:123], v[56:59]\n\t" \
  "ds_read_b64_tr_b16 v[92:93], %[b3] offset:40960\n\t" \
  "ds_read_b64_tr_b16 v[94:95], %[b3] offset:43008\n\t" \
  "s_waitcnt vmcnt(4)\n\t" \
  "s_waitcnt lgkmcnt(14)\n\t" \
  "v_mfma_f32_16x16x32_bf16 v[32:35], v[64:67], v[128:131], v[32:35]\n\t" \
  "v_mfma_f32_16x16x32_bf16 v[36:39], v[64:67], v[132:135], v[36:39]\n\t" \
  "ds_read_b64_tr_b16 v[64:65], %[b0] offset:49152\n\t" \
  "ds_read_b64_tr_b16 v[66:67], %[b0] offset:51200\n\t" \
  "s_waitcnt lgkmcnt(14)\n\t" \
  "v_mfma_f32_16x16x32_bf16 v[44:47], v[68:71], v[132:135], v[44:47]\n\t" \
  "v_mfma_f32_16x16x32_bf16 v[40:43], v[68:71], v[128:131], v[40:43]\n\t" \
  "ds_read_b64_tr_b16 v[68:69], %[b1] offset:49152\n\t" \
  "ds_read_b64_tr_b16 v[70:71], %[b1] offset:51200\n\t" \
  "s_waitcnt lgkmcnt(14)\n\t" \
  "v_mfma_f32_16x16x32_bf16 v[48:51], v[72:75], v[128:131], v[48:51]\n\t" \
  "v_mfma_f32_16x16x32_bf16 v[52:55], v[72:75], v[132:135], v[52:55]\n\t" \
  "ds_read_b64_tr_b16 v[72:73], %[b2] offset:49152\n\t" \
  "ds_read_b64_tr_b16 v[74:75], %[b2] offset:51200\n\t" \
  "s_waitcnt lgkmcnt(14)\n\t" \
  "v_mfma_f32_16x16x32_bf16 v[60:63], v[76:79], v[132:135], v[60:63]\n\t" \
  "v_mfma_f32_16x16x32_bf16 v[56:59], v[76:79], v[128:131], v[56:59]\n\t" \
  "ds_read_b64_tr_b16 v[76:77], %[b3] offset:49152\n\t" \
  "ds_read_b64_tr_b16 v[78:79], %[b3] offset:51200\n\t" \
  "s_waitcnt lgkmcnt(14)\n\t" \
  "v_mfma_f32_16x16x32_bf16 v[32:35], v[80:83], v[136:139], v[32:35]\n\t" \
  "v_mfma_f32_16x16x32_bf16 v[36:39], v[80:83], v[140:143], v[36:39]\n\t" \
  "ds_read_b64_tr_b16 v[80:81], %[b0] offset:57344\n\t" \
  "ds_read_b64_tr_b16 v[82:83], %[b0] offset:59392\n\t" \
  "s_waitcnt lgkmcnt(14)\n\t" \
  "v_mfma_f32_16x16x32_bf16 v[44:47], v[84:87], v[140:143], v[44:47]\n\t" \
  "v_mfma_f32_16x16x32_bf16 v[40:43], v[84:87], v[136:139], v[40:43]\n\t" \
  "ds_read_b64_tr_b16 v[84:85], %[b1] offset:57344\n\t" \
  "ds_read_b64_tr_b16 v[86:87], %[b1] offset:59392\n\t" \
  "s_waitcnt lgkmcnt(14)\n\t" \
  "v_mfma_f32_16x16x32_bf16 v[48:51], v[88:91], v[136:139], v[48:51]\n\t" \
  "v_mfma_f32_16x16x32_bf16 v[52:55], v[88:91], v[140:143], v[52:55]\n\t" \
  "ds_read_b64_tr_b16 v[88:89], %[b2] offset:57344\n\t" \
  "ds_read_b64_tr_b16 v[90:91], %[b2] offset:59392\n\t" \
  "s_waitcnt lgkmcnt(14)\n\t" \
  "v_mfma_f32_16x16x32_bf16 v[60:63], v[92:95], v[140:143], v[60:63]\n\t" \
  "v_mfma_f32_16x16x32_bf16 v[56:59], v[92:95], v[136:139], v[56:59]\n\t" \
  "ds_read_b64_tr_b16 v[92:93], %[b3] offset:57344\n\t" \
  "ds_read_b64_tr_b16 v[94:95], %[b3] offset:59392\n\t" \
  "s_waitcnt vmcnt(0)\n\t" \
  "s_waitcnt lgkmcnt(14)\n\t" \
  "v_mfma_f32_16x16x32_bf16 v[32:35], v[64:67], v[144:147], v[32:35]\n\t" \
  "v_mfma_f32_16x16x32_bf16 v[36:39], v[64:67], v[148:151], v[36:39]\n\t" \
  "s_waitcnt lgkmcnt(12)\n\t" \
  "v_mfma_f32_16x16x32_bf16 v[44:47], v[68:71], v[148:151], v[44:47]\n\t" \
  "v_mfma_f32_16x16x32_bf16 v[40:43], v[68:71], v[144:147], v[40:43]\n\t" \
  "s_waitcnt lgkmcnt(10)\n\t" \
  "v_mfma_f32_16x16x32_bf16 v[48:51], v[72:75], v[144:147], v[48:51]\n\t" \
  "v_mfma_f32_16x16x32_bf16 v[52:55], v[72:75], v[148:151], v[52:55]\n\t" \
  "s_waitcnt lgkmcnt(8)\n\t" \
  "v_mfma_f32_16x16x32_bf16 v[60:63], v[76:79], v[148:151], v[60:63]\n\t" \
  "v_mfma_f32_16x16x32_bf16 v[56:59], v[76:79], v[144:147], v[56:59]\n\t" \
  "s_waitcnt lgkmcnt(6)\n\t" \
  "v_mfma_f32_16x16x32_bf16 v[32:35], v[80:83], v[152:155], v[32:35]\n\t" \
  "v_mfma_f32_16x16x32_bf16 v[36:39], v[80:83], v[156:159], v[36:39]\n\t" \
  "s_waitcnt lgkmcnt(4)\n\t" \
  "v_mfma_f32_16x16x32_bf16 v[44:47], v[84:87], v[156:159], v[44:47]\n\t" \
  "v_mfma_f32_16x16x32_bf16 v[40:43], v[84:87], v[152:155], v[40:43]\n\t" \
  "s_waitcnt lgkmcnt(2)\n\t" \
  "v_mfma_f32_16x16x32_bf16 v[48:51], v[88:91], v[152:155], v[48:51]\n\t" \
  "v_mfma_f32_16x16x32_bf16 v[52:55], v[88:91], v[156:159], v[52:55]\n\t" \
  "s_waitcnt lgkmcnt(0)\n\t" \
  "v_mfma_f32_16x16x32_bf16 v[60:63], v[92:95], v[156:159], v[60:63]\n\t" \
  "v_mfma_f32_16x16x32_bf16 v[56:59], v[92:95], v[152:155], v[56:59]\n\t" \
  "s_nop 7\n\t" \
  ""
#define DN_ASM_LOOP \
  "buffer_load_dwordx4 v[128:131], %[vt0], %[srdT], %[sT] offen sc1\n\t" \
  "buffer_load_dwordx4 v[132:135], %[vt1], %[srdT], %[sT] offen sc1\n\t" \
  "s_add_u32 %[sT], %[sT], 0x800\n\t" \
  "buffer_load_dwordx4 v[136:139], %[vt0], %[srdT], %[sT] offen sc1\n\t" \
  "buffer_load_dwordx4 v[140:143], %[vt1], %[srdT], %[sT] offen sc1\n\t" \
  "s_add_u32 %[sT], %[sT], 0x800\n\t" \
  "s_barrier\n\t" \
  "buffer_load_dwordx4 v[144:147], %[vt0], %[srdT], %[sT] offen sc1\n\t" \
  "buffer_load_dwordx4 v[148:151], %[vt1], %[srdT], %[sT] offen sc1\n\t" \
  "s_add_u32 %[sT], %[sT], 0x800\n\t" \
  "buffer_load_dwordx4 v[152:155], %[vt0], %[srdT], %[sT] offen sc1\n\t" \
  "buffer_load_dwordx4 v[156:159], %[vt1], %[srdT], %[sT] offen sc1\n\t" \
  "s_add_u32 %[sT], %[sT], 0x800\n\t" \
  "v_mov_b64 v[32:33], 0\n\t" \
  "v_mov_b64 v[34:35], 0\n\t" \
  "v_mov_b64 v[36:37], 0\n\t" \
  "v_mov_b64 v[38:39], 0\n\t" \
  "v_mov_b64 v[40:41], 0\n\t" \
  "v_mov_b64 v[42:43], 0\n\t" \
  "v_mov_b64 v[44:45], 0\n\t" \
  "v_mov_b64 v[46:47], 0\n\t" \
  "v_mov_b64 v[48:49], 0\n\t" \
  "v_mov_b64 v[50:51], 0\n\t" \
  "v_mov_b64 v[52:53], 0\n\t" \
  "v_mov_b64 v[54:55], 0\n\t" \
  "v_mov_b64 v[56:57], 0\n\t" \
  "v_mov_b64 v[58:59], 0\n\t" \
  "v_mov_b64 v[60:61], 0\n\t" \
  "v_mov_b64 v[62:63], 0\n\t" \
  "v_mov_b64 v[64:65], 0\n\t" \
  "v_mov_b64 v[66:67], 0\n\t" \
  "v_mov_b64 v[68:69], 0\n\t" \
  "v_mov_b64 v[70:71], 0\n\t" \
  "v_mov_b64 v[72:73], 0\n\t" \
  "v_mov_b64 v[74:75], 0\n\t" \
  "v_mov_b64 v[76:77], 0\n\t" \
  "v_mov_b64 v[78:79], 0\n\t" \
  "v_mov_b64 v[80:81], 0\n\t" \
  "v_mov_b64 v[82:83], 0\n\t" \
  "v_mov_b64 v[84:85], 0\n\t" \
  "v_mov_b64 v[86:87], 0\n\t" \
  "v_mov_b64 v[88:89], 0\n\t" \
  "v_mov_b64 v[90:91], 0\n\t" \
  "v_mov_b64 v[92:93], 0\n\t" \
  "v_mov_b64 v[94:95], 0\n\t" \
  "s_waitcnt vmcnt(8)\n\t" \
  "s_barrier\n\t" \
  "ds_read_b64_tr_b16 v[96:97], %[a0] offset:0\n\t" \
  "ds_read_b64_tr_b16 v[98:99], %[a0] offset:4096\n\t" \
  "ds_read_b64_tr_b16 v[100:101], %[a1] offset:0\n\t" \
  "ds_read_b64_tr_b16 v[102:103], %[a1] offset:4096\n\t" \
  "ds_read_b64_tr_b16 v[104:105], %[a2] offset:0\n\t" \
  "ds_read_b64_tr_b16 v[106:107], %[a2] offset:4096\n\t" \
  "ds_read_b64_tr_b16 v[108:109], %[a3] offset:0\n\t" \
  "ds_read_b64_tr_b16 v[110:111], %[a3] offset:4096\n\t" \
  "ds_read_b64_tr_b16 v[112:113], %[a4] offset:0\n\t" \
  "ds_read_b64_tr_b16 v[114:115], %[a4] offset:4096\n\t" \
  "ds_read_b64_tr_b16 v[116:117], %[a5] offset:0\n\t" \
  "ds_read_b64_tr_b16 v[118:119], %[a5] offset:4096\n\t" \
  "ds_read_b64_tr_b16 v[120:121], %[a6] offset:0\n\t" \
  "ds_read_b64_tr_b16 v[122:123], %[a6] offset:4096\n\t" \
  "ds_read_b64_tr_b16 v[124:125], %[a7] offset:0\n\t" \
  "ds_read_b64_tr_b16 v[126:127], %[a7] offset:4096\n\t" \
  "s_xor_b32 %[dW], %[dW], 0x10000\n\t" \
  "s_mov_b32 %[cnt], 5\n\t" \
  "1:\n\t" \
  "s_waitcnt vmcnt(6)\n\t" \
  "s_barrier\n\t" \
  "s_waitcnt lgkmcnt(14)\n\t" \
  "v_mfma_f32_16x16x32_bf16 v[32:35], v[96:99], v[128:131], v[32:35]\n\t" \
  "s_add_u32 m0, %[dW], 0\n\t" \
  "v_mfma_f32_16x16x32_bf16 v[36:39], v[96:99], v[132:135], v[36:39]\n\t" \
  "ds_read_b64_tr_b16 v[96:97], %[a0] offset:16384\n\t" \
  "ds_read_b64_tr_b16 v[98:99], %[a0] offset:20480\n\t" \
  "s_waitcnt lgkmcnt(14)\n\t" \
  "v_mfma_f32_16x16x32_bf16 v[44:47], v[100:103], v[132:135], v[44:47]\n\t" \
  "buffer_load_dwordx4 %[vW], %[srdW], %[sW] offen lds\n\t" \
  "v_mfma_f32_16x16x32_bf16 v[40:43], v[100:103], v[128:131], v[40:43]\n\t" \
  "ds_read_b64_tr_b16 v[100:101], %[a1] offset:16384\n\t" \
  "ds_read_b64_tr_b16 v[102:103], %[a1] offset:20480\n\t" \
  "s_waitcnt lgkmcnt(14)\n\t" \
  "v_mfma_f32_16x16x32_bf16 v[48:51], v[104:107], v[128:131], v[48:51]\n\t" \
  "v_mfma_f32_16x16x32_bf16 v[52:55], v[104:107], v[132:135], v[52:55]\n\t" \
  "ds_read_b64_tr_b16 v[104:105], %[a2] offset:16384\n\t" \
  "ds_read_b64_tr_b16 v[106:107], %[a2] offset:20480\n\t" \
  "s_waitcnt lgkmcnt(14)\n\t" \
  "v_mfma_f32_16x16x32_bf16 v[60:63], v[108:111], v[132:135], v[60:63]\n\t" \
  "s_add_u32 m0, %[dW], 1024\n\t" \
  "v_mfma_f32_16x16x32_bf16 v[56:59], v[108:111], v[128:131], v[56:59]\n\t" \
  "ds_read_b64_tr_b16 v[108:109], %[a3] offset:16384\n\t" \
  "ds_read_b64_tr_b16 v[110:111], %[a3] offset:20480\n\t" \
  "s_waitcnt lgkmcnt(14)\n\t" \
  "v_mfma_f32_16x16x32_bf16 v[64:67], v[112:115], v[128:131], v[64:67]\n\t" \
  "buffer_load_dwordx4 %[vW2], %[srdW], %[sW] offen lds\n\t" \
  "s_add_u32 %[sW], %[sW], 131072\n\t" \
  "v_mfma_f32_16x16x32_bf16 v[68:71], v[112:115], v[132:135], v[68:71]\n\t" \
  "ds_read_b64_tr_b16 v[112:113], %[a4] offset:16384\n\t" \
  "ds_read_b64_tr_b16 v[114:115], %[a4] offset:20480\n\t" \
  "s_waitcnt lgkmcnt(14)\n\t" \
  "v_mfma_f32_16x16x32_bf16 v[76:79], v[116:119], v[132:135], v[76:79]\n\t" \
  "v_mfma_f32_16x16x32_bf16 v[72:75], v[116:119], v[128:131], v[72:75]\n\t" \
  "ds_read_b64_tr_b16 v[116:117], %[a5] offset:16384\n\t" \
  "ds_read_b64_tr_b16 v[118:119], %[a5] offset:20480\n\t" \
  "s_waitcnt lgkmcnt(14)\n\t" \
  "v_mfma_f32_16x16x32_bf16 v[80:83], v[120:123], v[128:131], v[80:83]\n\t" \
  "v_mfma_f32_16x16x32_bf16 v[84:87], v[120:123], v[132:135], v[84:87]\n\t" \
  "ds_read_b64_tr_b16 v[120:121], %[a6] offset:16384\n\t" \
  "ds_read_b64_tr_b16 v[122:123], %[a6] offset:20480\n\t" \
  "s_waitcnt lgkmcnt(14)\n\t" \
  "v_mfma_f32_16x16x32_bf16 v[92:95], v[124:127], v[132:135], v[92:95]\n\t" \
  "buffer_load_dwordx4 v[132:135], %[vt1], %[srdT], %[sT] offen sc1\n\t" \
  "v_mfma_f32_16x16x32_bf16 v[88:91], v[124:127], v[128:131], v[88:91]\n\t" \
  "ds_read_b64_tr_b16 v[124:125], %[a7] offset:16384\n\t" \
  "ds_read_b64_tr_b16 v[126:127], %[a7] offset:20480\n\t" \
  "buffer_load_dwordx4 v[128:131], %[vt0], %[srdT], %[sT] offen sc1\n\t" \
  "s_add_u32 %[sT], %[sT], 0x800\n\t" \
  "s_waitcnt vmcnt(8)\n\t" \
  "s_waitcnt lgkmcnt(14)\n\t" \
  "v_mfma_f32_16x16x32_bf16 v[32:35], v[96:99], v[136:139], v[32:35]\n\t" \
  "s_add_u32 m0, %[dW], 16384\n\t" \
  "v_mfma_f32_16x16x32_bf16 v[36:39], v[96:99], v[140:143], v[36:39]\n\t" \
  "ds_read_b64_tr_b16 v[96:97], %[a0] offset:32768\n\t" \
  "ds_read_b64_tr_b16 v[98:99], %[a0] offset:36864\n\t" \
  "s_waitcnt lgkmcnt(14)\n\t" \
  "v_mfma_f32_16x16x32_bf16 v[44:47], v[100:103], v[140:143], v[44:47]\n\t" \
  "buffer_load_dwordx4 %[vW], %[srdW], %[sW] offen lds\n\t" \
  "v_mfma_f32_16x16x32_bf16 v[40:43], v[100:103], v[136:139], v[40:43]\n\t" \
  "ds_read_b64_tr_b16 v[100:101], %[a1] offset:32768\n\t" \
  "ds_read_b64_tr_b16 v[102:103], %[a1] offset:36864\n\t" \
  "s_waitcnt lgkmcnt(14)\n\t" \
  "v_mfma_f32_16x16x32_bf16 v[48:51], v[104:107], v[136:139], v[48:51]\n\t" \
  "v_mfma_f32_16x16x32_bf16 v[52:55], v[104:107], v[140:143], v[52:55]\n\t" \
  "ds_read_b64_tr_b16 v[104:105], %[a2] offset:32768\n\t" \
  "ds_read_b64_tr_b16 v[106:107], %[a2] offset:36864\n\t" \
  "s_waitcnt lgkmcnt(14)\n\t" \
  "v_mfma_f32_16x16x32_bf16 v[60:63], v[108:111], v[140:143], v[60:63]\n\t" \
  "s_add_u32 m0, %[dW], 17408\n\t" \
  "v_mfma_f32_16x16x32_bf16 v[56:59], v[108:111], v[136:139], v[56:59]\n\t" \
  "ds_read_b64_tr_b16 v[108:109], %[a3] offset:32768\n\t" \
  "ds_read_b64_tr_b16 v[110:111], %[a3] offset:36864\n\t" \
  "s_waitcnt lgkmcnt(14)\n\t" \
  "v_mfma_f32_16x16x32_bf16 v[64:67], v[112:115], v[136:139], v[64:67]\n\t" \
  "buffer_load_dwordx4 %[vW2], %[srdW], %[sW] offen lds\n\t" \
  "s_add_u32 %[sW], %[sW], 131072\n\t" \
  "v_mfma_f32_16x16x32_bf16 v[68:71], v[112:115], v[140:143], v[68:71]\n\t" \
  "ds_read_b64_tr_b16 v[112:113], %[a4] offset:32768\n\t" \
  "ds_read_b64_tr_b16 v[114:115], %[a4] offset:36864\n\t" \
  "s_waitcnt lgkmcnt(14)\n\t" \
  "v_mfma_f32_16x16x32_bf16 v[76:79], v[116:119], v[140:143], v[76:79]\n\t" \
  "v_mfma_f32_16x16x32_bf16 v[72:75], v[116:119], v[136:139], v[72:75]\n\t" \
  "ds_read_b64_tr_b16 v[116:117], %[a5] offset:32768\n\t" \
  "ds_read_b64_tr_b16 v[118:119], %[a5] offset:36864\n\t" \
  "s_waitcnt lgkmcnt(14)\n\t" \
  "v_mfma_f32_16x16x32_bf16 v[80:83], v[120:123], v[136:139], v[80:83]\n\t" \
  "v_mfma_f32_16x16x32_bf16 v[84:87], v[120:123], v[140:143], v[84:87]\n\t" \
  "ds_read_b64_tr_b16 v[120:121], %[a6] offset:32768\n\t" \
  "ds_read_b64_tr_b16 v[122:123], %[a6] offset:36864\n\t" \
  "s_waitcnt lgkmcnt(14)\n\t" \
  "v_mfma_f32_16x16x32_bf16 v[92:95], v[124:127], v[140:143], v[92:95]\n\t" \
  "buffer_load_dwordx4 v[140:143], %[vt1], %[srdT], %[sT] offen sc1\n\t" \
  "v_mfma_f32_16x16x32_bf16 v[88:91], v[124:127], v[136:139], v[88:91]\n\t" \
  "ds_read_b64_tr_b16 v[124:125], %[a7] offset:32768\n\t" \
  "ds_read_b64_tr_b16 v[126:127], %[a7] offset:36864\n\t" \
  "buffer_load_dwordx4 v[136:139], %[vt0], %[srdT], %[sT] offen sc1\n\t" \
  "s_add_u32 %[sT], %[sT], 0x800\n\t" \
  "s_waitcnt vmcnt(6)\n\t" \
  "s_barrier\n\t" \
  "s_waitcnt lgkmcnt(14)\n\t" \
  "v_mfma_f32_16x16x32_bf16 v[32:35], v[96:99], v[144:147], v[32:35]\n\t" \
  "s_add_u32 m0, %[dW], 32768\n\t" \
  "v_mfma_f32_16x16x32_bf16 v[36:39], v[96:99], v[148:151], v[36:39]\n\t" \
  "ds_read_b64_tr_b16 v[96:97], %[a0] offset:49152\n\t" \
  "ds_read_b64_tr_b16 v[98:99], %[a0] offset:53248\n\t" \
  "s_waitcnt lgkmcnt(14)\n\t" \
  "v_mfma_f32_16x16x32_bf16 v[44:47], v[100:103], v[148:151], v[44:47]\n\t" \
  "buffer_load_dwordx4 %[vW], %[srdW], %[sW] offen lds\n\t" \
  "v_mfma_f32_16x16x32_bf16 v[40:43], v[100:103], v[144:147], v[40:43]\n\t" \
  "ds_read_b64_tr_b16 v[100:101], %[a1] offset:49152\n\t" \
  "ds_read_b64_tr_b16 v[102:103], %[a1] offset:53248\n\t" \
  "s_waitcnt lgkmcnt(14)\n\t" \
  "v_mfma_f32_16x16x32_bf16 v[48:51], v[104:107], v[144:147], v[48:51]\n\t" \
  "v_mfma_f32_16x16x32_bf16 v[52:55], v[104:107], v[148:151], v[52:55]\n\t" \
  "ds_read_b64_tr_b16 v[104:105], %[a2] offset:49152\n\t" \
  "ds_read_b64_tr_b16 v[106:107], %[a2] offset:53248\n\t" \
  "s_waitcnt lgkmcnt(14)\n\t" \
  "v_mfma_f32_16x16x32_bf16 v[60:63], v[108:111], v[148:151], v[60:63]\n\t" \
  "s_add_u32 m0, %[dW], 33792\n\t" \
  "v_mfma_f32_16x16x32_bf16 v[56:59], v[108:111], v[144:147], v[56:59]\n\t" \
  "ds_read_b64_tr_b16 v[108:109], %[a3] offset:49152\n\t" \
  "ds_read_b64_tr_b16 v[110:111], %[a3] offset:53248\n\t" \
  "s_waitcnt lgkmcnt(14)\n\t" \
  "v_mfma_f32_16x16x32_bf16 v[64:67], v[112:115], v[144:147], v[64:67]\n\t" \
  "buffer_load_dwordx4 %[vW2], %[srdW], %[sW] offen lds\n\t" \
  "s_add_u32 %[sW], %[sW], 131072\n\t" \
  "v_mfma_f32_16x16x32_bf16 v[68:71], v[112:115], v[148:151], v[68:71]\n\t" \
  "ds_read_b64_tr_b16 v[112:113], %[a4] offset:49152\n\t" \
  "ds_read_b64_tr_b16 v[114:115], %[a4] offset:53248\n\t" \
  "s_waitcnt lgkmcnt(14)\n\t" \
  "v_mfma_f32_16x16x32_bf16 v[76:79], v[116:119], v[148:151], v[76:79]\n\t" \
  "v_mfma_f32_16x16x32_bf16 v[72:75], v[116:119], v[144:147], v[72:75]\n\t" \
  "ds_read_b64_tr_b16 v[116:117], %[a5] offset:49152\n\t" \
  "ds_read_b64_tr_b16 v[118:119], %[a5] offset:53248\n\t" \
  "s_waitcnt lgkmcnt(14)\n\t" \
  "v_mfma_f32_16x16x32_bf16 v[80:83], v[120:123], v[144:147], v[80:83]\n\t" \
  "v_mfma_f32_16x16x32_bf16 v[84:87], v[120:123], v[148:151], v[84:87]\n\t" \
  "ds_read_b64_tr_b16 v[120:121], %[a6] offset:49152\n\t" \
  "ds_read_b64_tr_b16 v[122:123], %[a6] offset:53248\n\t" \
  "s_waitcnt lgkmcnt(14)\n\t" \
  "v_mfma_f32_16x16x32_bf16 v[92:95], v[124:127], v[148:151], v[92:95]\n\t" \
  "buffer_load_dwordx4 v[148:151], %[vt1], %[srdT], %[sT] offen sc1\n\t" \
  "v_mfma_f32_16x16x32_bf16 v[88:91], v[124:127], v[144:147], v[88:91]\n\t" \
  "ds_read_b64_tr_b16 v[124:125], %[a7] offset:49152\n\t" \
  "ds_read_b64_tr_b16 v[126:127], %[a7] offset:53248\n\t" \
  "buffer_load_dwordx4 v[144:147], %[vt0], %[srdT], %[sT] offen sc1\n\t" \
  "s_add_u32 %[sT], %[sT], 0x800\n\t" \
  "s_waitcnt vmcnt(8)\n\t" \
  "s_waitcnt lgkmcnt(14)\n\t" \
  "v_mfma_f32_16x16x32_bf16 v[32:35], v[96:99], v[152:155], v[32:35]\n\t" \
  "s_add_u32 m0, %[dW], 49152\n\t" \
  "v_xor_b32 %[a0], 0x10000, %[a0]\n\t" \
  "v_mfma_f32_16x16x32_bf16 v[36:39], v[96:99], v[156:159], v[36:39]\n\t" \
  "ds_read_b64_tr_b16 v[96:97], %[a0] offset:0\n\t" \
  "ds_read_b64_tr_b16 v[98:99], %[a0] offset:4096\n\t" \
  "s_waitcnt lgkmcnt(14)\n\t" \
  "v_mfma_f32_16x16x32_bf16 v[44:47], v[100:103], v[156:159], v[44:47]\n\t" \
  "buffer_load_dwordx4 %[vW], %[srdW], %[sW] offen lds\n\t" \
  "v_xor_b32 %[a1], 0x10000, %[a1]\n\t" \
  "v_mfma_f32_16x16x32_bf16 v[40:43], v[100:103], v[152:155], v[40:43]\n\t" \
  "ds_read_b64_tr_b16 v[100:101], %[a1] offset:0\n\t" \
  "ds_read_b64_tr_b16 v[102:103], %[a1] offset:4096\n\t" \
  "s_waitcnt lgkmcnt(14)\n\t" \
  "v_mfma_f32_16x16x32_bf16 v[48:51], v[104:107], v[152:155], v[48:51]\n\t" \
  "v_xor_b32 %[a2], 0x10000, %[a2]\n\t" \
  "v_mfma_f32_16x16x32_bf16 v[52:55], v[104:107], v[156:159], v[52:55]\n\t" \
  "ds_read_b64_tr_b16 v[104:105], %[a2] offset:0\n\t" \
  "ds_read_b64_tr_b16 v[106:107], %[a2] offset:4096\n\t" \
  "s_waitcnt lgkmcnt(14)\n\t" \
  "v_mfma_f32_16x16x32_bf16 v[60:63], v[108:111], v[156:159], v[60:63]\n\t" \
  "s_add_u32 m0, %[dW], 50176\n\t" \
  "v_xor_b32 %[a3], 0x10000, %[a3]\n\t" \
  "v_mfma_f32_16x16x32_bf16 v[56:59], v[108:111], v[152:155], v[56:59]\n\t" \
  "ds_read_b64_tr_b16 v[108:109], %[a3] offset:0\n\t" \
  "ds_read_b64_tr_b16 v[110:111], %[a3] offset:4096\n\t" \
  "s_waitcnt lgkmcnt(14)\n\t" \
  "v_mfma_f32_16x16x32_bf16 v[64:67], v[112:115], v[152:155], v[64:67]\n\t" \
  "buffer_load_dwordx4 %[vW2], %[srdW], %[sW] offen lds\n\t" \
  "s_add_u32 %[sW], %[sW], 131072\n\t" \
  "v_xor_b32 %[a4], 0x10000, %[a4]\n\t" \
  "v_mfma_f32_16x16x32_bf16 v[68:71], v[112:115], v[156:159], v[68:71]\n\t" \
  "ds_read_b64_tr_b16 v[112:113], %[a4] offset:0\n\t" \
  "ds_read_b64_tr_b16 v[114:115], %[a4] offset:4096\n\t" \
  "s_waitcnt lgkmcnt(14)\n\t" \
  "v_mfma_f32_16x16x32_bf16 v[76:79], v[116:119], v[156:159], v[76:79]\n\t" \
  "v_xor_b32 %[a5], 0x10000, %[a5]\n\t" \
  "v_mfma_f32_16x16x32_bf16 v[72:75], v[116:119], v[152:155], v[72:75]\n\t" \
  "ds_read_b64_tr_b16 v[116:117], %[a5] offset:0\n\t" \
  "ds_read_b64_tr_b16 v[118:119], %[a5] offset:4096\n\t" \
  "s_waitcnt lgkmcnt(14)\n\t" \
  "v_mfma_f32_16x16x32_bf16 v[80:83], v[120:123], v[152:155], v[80:83]\n\t" \
  "v_xor_b32 %[a6], 0x10000, %[a6]\n\t" \
  "v_mfma_f32_16x16x32_bf16 v[84:87], v[120:123], v[156:159], v[84:87]\n\t" \
  "ds_read_b64_tr_b16 v[120:121], %[a6] offset:0\n\t" \
  "ds_read_b64_tr_b16 v[122:123], %[a6] offset:4096\n\t" \
  "s_waitcnt lgkmcnt(14)\n\t" \
  "v_mfma_f32_16x16x32_bf16 v[92:95], v[124:127], v[156:159], v[92:95]\n\t" \
  "v_xor_b32 %[a7], 0x10000, %[a7]\n\t" \
  "buffer_load_dwordx4 v[156:159], %[vt1], %[srdT], %[sT] offen sc1\n\t" \
  "v_mfma_f32_16x16x32_bf16 v[88:91], v[124:127], v[152:155], v[88:91]\n\t" \
  "ds_read_b64_tr_b16 v[124:125], %[a7] offset:0\n\t" \
  "ds_read_b64_tr_b16 v[126:127], %[a7] offset:4096\n\t" \
  "buffer_load_dwordx4 v[152:155], %[vt0], %[srdT], %[sT] offen sc1\n\t" \
  "s_add_u32 %[sT], %[sT], 0x800\n\t" \
  "s_xor_b32 %[dW], %[dW], 0x10000\n\t" \
  "s_sub_u32 %[cnt], %[cnt], 1\n\t" \
  "s_cmp_lg_u32 %[cnt], 0\n\t" \
  "s_cbranch_scc1 1b\n\t" \
  "v_xor_b32 v176, 0x10000, %[a0]\n\t" \
  "v_xor_b32 v177, 0x10000, %[a1]\n\t" \
  "v_xor_b32 v178, 0x10000, %[a2]\n\t" \
  "v_xor_b32 v179, 0x10000, %[a3]\n\t" \
  "v_xor_b32 v180, 0x10000, %[a4]\n\t" \
  "v_xor_b32 v181, 0x10000, %[a5]\n\t" \
  "v_xor_b32 v182, 0x10000, %[a6]\n\t" \
  "v_xor_b32 v183, 0x10000, %[a7]\n\t" \
  "s_waitcnt vmcnt(6)\n\t" \
  "s_barrier\n\t" \
  "s_waitcnt lgkmcnt(0)\n\t" \
  "v_mfma_f32_16x16x32_bf16 v[64:67], v[112:115], v[128:131], v[64:67]\n\t" \
  "s_add_u32 m0, %[dW], 0\n\t" \
  "v_mfma_f32_16x16x32_bf16 v[68:71], v[112:115], v[132:135], v[68:71]\n\t" \
  "ds_read_b64_tr_b16 v[112:113], %[a4] offset:16384\n\t" \
  "ds_read_b64_tr_b16 v[114:115], %[a4] offset:20480\n\t" \
  "s_nop 0\n\t" \
  "v_mfma_f32_16x16x32_bf16 v[76:79], v[116:119], v[132:135], v[76:79]\n\t" \
  "buffer_load_dwordx4 %[vW], %[srdW], %[sW] offen lds\n\t" \
  "v_mfma_f32_16x16x32_bf16 v[72:75], v[116:119], v[128:131], v[72:75]\n\t" \
  "ds_read_b64_tr_b16 v[116:117], %[a5] offset:16384\n\t" \
  "ds_read_b64_tr_b16 v[118:119], %[a5] offset:20480\n\t" \
  "s_nop 0\n\t" \
  "v_mfma_f32_16x16x32_bf16 v[80:83], v[120:123], v[128:131], v[80:83]\n\t" \
  "s_add_u32 m0, %[dW], 1024\n\t" \
  "v_mfma_f32_16x16x32_bf16 v[84:87], v[120:123], v[132:135], v[84:87]\n\t" \
  "ds_read_b64_tr_b16 v[120:121], %[a6] offset:16384\n\t" \
  "ds_read_b64_tr_b16 v[122:123], %[a6] offset:20480\n\t" \
  "s_nop 0\n\t" \
  "v_mfma_f32_16x16x32_bf16 v[92:95], v[124:127], v[132:135], v[92:95]\n\t" \
  "buffer_load_dwordx4 %[vW2], %[srdW], %[sW] offen lds\n\t" \
  "v_mfma_f32_16x16x32_bf16 v[88:91], v[124:127], v[128:131], v[88:91]\n\t" \
  "s_add_u32 %[sW], %[sW], 131072\n\t" \
  "ds_read_b64_tr_b16 v[124:125], %[a7] offset:16384\n\t" \
  "ds_read_b64_tr_b16 v[126:127], %[a7] offset:20480\n\t" \
  "buffer_load_dwordx4 v[184:187], %[vt0], %[srdT], %[sT] offen sc1\n\t" \
  "buffer_load_dwordx4 v[188:191], %[vt1], %[srdT], %[sT] offen sc1\n\t" \
  "s_add_u32 %[sT], %[sT], 0x800\n\t" \
  "s_waitcnt vmcnt(8)\n\t" \
  "s_waitcnt lgkmcnt(6)\n\t" \
  "v_mfma_f32_16x16x32_bf16 v[64:67], v[112:115], v[136:139], v[64:67]\n\t" \
  "s_add_u32 m0, %[dW], 16384\n\t" \
  "v_mfma_f32_16x16x32_bf16 v[68:71], v[112:115], v[140:143], v[68:71]\n\t" \
  "ds_read_b64_tr_b16 v[112:113], %[a4] offset:32768\n\t" \
  "ds_read_b64_tr_b16 v[114:115], %[a4] offset:36864\n\t" \
  "s_waitcnt lgkmcnt(6)\n\t" \
  "v_mfma_f32_16x16x32_bf16 v[76:79], v[116:119], v[140:143], v[76:79]\n\t" \
  "buffer_load_dwordx4 %[vW], %[srdW], %[sW] offen lds\n\t" \
  "v_mfma_f32_16x16x32_bf16 v[72:75], v[116:119], v[136:139], v[72:75]\n\t" \
  "ds_read_b64_tr_b16 v[116:117], %[a5] offset:32768\n\t" \
  "ds_read_b64_tr_b16 v[118:119], %[a5] offset:36864\n\t" \
  "s_waitcnt lgkmcnt(6)\n\t" \
  "v_mfma_f32_16x16x32_bf16 v[80:83], v[120:123], v[136:139], v[80:83]\n\t" \
  "s_add_u32 m0, %[dW], 17408\n\t" \
  "v_mfma_f32_16x16x32_bf16 v[84:87], v[120:123], v[140:143], v[84:87]\n\t" \
  "ds_read_b64_tr_b16 v[120:121], %[a6] offset:32768\n\t" \
  "ds_read_b64_tr_b16 v[122:123], %[a6] offset:36864\n\t" \
  "s_waitcnt lgkmcnt(6)\n\t" \
  "v_mfma_f32_16x16x32_bf16 v[92:95], v[124:127], v[140:143], v[92:95]\n\t" \
  "buffer_load_dwordx4 %[vW2], %[srdW], %[sW] offen lds\n\t" \
  "v_mfma_f32_16x16x32_bf16 v[88:91], v[124:127], v[136:139], v[88:91]\n\t" \
  "s_add_u32 %[sW], %[sW], 131072\n\t" \
  "ds_read_b64_tr_b16 v[124:125], %[a7] offset:32768\n\t" \
  "ds_read_b64_tr_b16 v[126:127], %[a7] offset:36864\n\t" \
  "buffer_load_dwordx4 v[192:195], %[vt0], %[srdT], %[sT] offen sc1\n\t" \
  "buffer_load_dwordx4 v[196:199], %[vt1], %[srdT], %[sT] offen sc1\n\t" \
  "s_add_u32 %[sT], %[sT], 0x800\n\t" \
  "s_waitcnt vmcnt(6)\n\t" \
  "s_barrier\n\t" \
  "s_waitcnt lgkmcnt(6)\n\t" \
  "v_mfma_f32_16x16x32_bf16 v[64:67], v[112:115], v[144:147], v[64:67]\n\t" \
  "s_add_u32 m0, %[dW], 32768\n\t" \
  "v_mfma_f32_16x16x32_bf16 v[68:71], v[112:115], v[148:151], v[68:71]\n\t" \
  "ds_read_b64_tr_b16 v[112:113], %[a4] offset:49152\n\t" \
  "ds_read_b64_tr_b16 v[114:115], %[a4] offset:53248\n\t" \
  "s_waitcnt lgkmcnt(6)\n\t" \
  "v_mfma_f32_16x16x32_bf16 v[76:79], v[116:119], v[148:151], v[76:79]\n\t" \
  "buffer_load_dwordx4 %[vW], %[srdW], %[sW] offen lds\n\t" \
  "v_mfma_f32_16x16x32_bf16 v[72:75], v[116:119], v[144:147], v[72:75]\n\t" \
  "ds_read_b64_tr_b16 v[116:117], %[a5] offset:49152\n\t" \
  "ds_read_b64_tr_b16 v[118:119], %[a5] offset:53248\n\t" \
  "s_waitcnt lgkmcnt(6)\n\t" \
  "v_mfma_f32_16x16x32_bf16 v[80:83], v[120:123], v[144:147], v[80:83]\n\t" \
  "s_add_u32 m0, %[dW], 33792\n\t" \
  "v_mfma_f32_16x16x32_bf16 v[84:87], v[120:123], v[148:151], v[84:87]\n\t" \
  "ds_read_b64_tr_b16 v[120:121], %[a6] offset:49152\n\t" \
  "ds_read_b64_tr_b16 v[122:123], %[a6] offset:53248\n\t" \
  "s_waitcnt lgkmcnt(6)\n\t" \
  "v_mfma_f32_16x16x32_bf16 v[92:95], v[124:127], v[148:151], v[92:95]\n\t" \
  "buffer_load_dwordx4 %[vW2], %[srdW], %[sW] offen lds\n\t" \
  "v_mfma_f32_16x16x32_bf16 v[88:91], v[124:127], v[144:147], v[88:91]\n\t" \
  "s_add_u32 %[sW], %[sW], 131072\n\t" \
  "ds_read_b64_tr_b16 v[124:125], %[a7] offset:49152\n\t" \
  "ds_read_b64_tr_b16 v[126:127], %[a7] offset:53248\n\t" \
  "buffer_load_dwordx4 v[200:203], %[vt0], %[srdT], %[sT] offen sc1\n\t" \
  "buffer_load_dwordx4 v[204:207], %[vt1], %[srdT], %[sT] offen sc1\n\t" \
  "s_add_u32 %[sT], %[sT], 0x800\n\t" \
  "s_waitcnt vmcnt(8)\n\t" \
  "s_waitcnt lgkmcnt(6)\n\t" \
  "v_mfma_f32_16x16x32_bf16 v[64:67], v[112:115], v[152:155], v[64:67]\n\t" \
  "s_add_u32 m0, %[dW], 49152\n\t" \
  "v_mfma_f32_16x16x32_bf16 v[68:71], v[112:115], v[156:159], v[68:71]\n\t" \
  "ds_read_b64_tr_b16 v[112:113], v180 offset:0\n\t" \
  "ds_read_b64_tr_b16 v[114:115], v180 offset:4096\n\t" \
  "s_waitcnt lgkmcnt(6)\n\t" \
  "v_mfma_f32_16x16x32_bf16 v[76:79], v[116:119], v[156:159], v[76:79]\n\t" \
  "buffer_load_dwordx4 %[vW], %[srdW], %[sW] offen lds\n\t" \
  "v_mfma_f32_16x16x32_bf16 v[72:75], v[116:119], v[152:155], v[72:75]\n\t" \
  "ds_read_b64_tr_b16 v[116:117], v181 offset:0\n\t" \
  "ds_read_b64_tr_b16 v[118:119], v181 offset:4096\n\t" \
  "s_waitcnt lgkmcnt(6)\n\t" \
  "v_mfma_f32_16x16x32_bf16 v[80:83], v[120:123], v[152:155], v[80:83]\n\t" \
  "s_add_u32 m0, %[dW], 50176\n\t" \
  "v_mfma_f32_16x16x32_bf16 v[84:87], v[120:123], v[156:159], v[84:87]\n\t" \
  "ds_read_b64_tr_b16 v[120:121], v182 offset:0\n\t" \
  "ds_read_b64_tr_b16 v[122:123], v182 offset:4096\n\t" \
  "s_waitcnt lgkmcnt(6)\n\t" \
  "v_mfma_f32_16x16x32_bf16 v[92:95], v[124:127], v[156:159], v[92:95]\n\t" \
  "buffer_load_dwordx4 %[vW2], %[srdW], %[sW] offen lds\n\t" \
  "v_mfma_f32_16x16x32_bf16 v[88:91], v[124:127], v[152:155], v[88:91]\n\t" \
  "s_add_u32 %[sW], %[sW], 131072\n\t" \
  "ds_read_b64_tr_b16 v[124:125], v183 offset:0\n\t" \
  "ds_read_b64_tr_b16 v[126:127], v183 offset:4096\n\t" \
  "buffer_load_dwordx4 v[208:211], %[vt0], %[srdT], %[sT] offen sc1\n\t" \
  "buffer_load_dwordx4 v[212:215], %[vt1], %[srdT], %[sT] offen sc1\n\t" \
  "s_add_u32 %[sT], %[sT], 0x800\n\t" \
  "s_waitcnt vmcnt(6)\n\t" \
  "s_barrier\n\t" \
  "s_waitcnt lgkmcnt(6)\n\t" \
  "v_mfma_f32_16x16x32_bf16 v[64:67], v[112:115], v[184:187], v[64:67]\n\t" \
  "v_mfma_f32_16x16x32_bf16 v[68:71], v[112:115], v[188:191], v[68:71]\n\t" \
  "ds_read_b64_tr_b16 v[112:113], v180 offset:16384\n\t" \
  "ds_read_b64_tr_b16 v[114:115], v180 offset:20480\n\t" \
  "s_waitcnt lgkmcnt(6)\n\t" \
  "v_mfma_f32_16x16x32_bf16 v[76:79], v[116:119], v[188:191], v[76:79]\n\t" \
  "v_mfma_f32_16x16x32_bf16 v[72:75], v[116:119], v[184:187], v[72:75]\n\t" \
  "ds_read_b64_tr_b16 v[116:117], v181 offset:16384\n\t" \
  "ds_read_b64_tr_b16 v[118:119], v181 offset:20480\n\t" \
  "s_waitcnt lgkmcnt(6)\n\t" \
  "v_mfma_f32_16x16x32_bf16 v[80:83], v[120:123], v[184:187], v[80:83]\n\t" \
  "v_mfma_f32_16x16x32_bf16 v[84:87], v[120:123], v[188:191], v[84:87]\n\t" \
  "ds_read_b64_tr_b16 v[120:121], v182 offset:16384\n\t" \
  "ds_read_b64_tr_b16 v[122:123], v182 offset:20480\n\t" \
  "s_waitcnt lgkmcnt(6)\n\t" \
  "v_mfma_f32_16x16x32_bf16 v[92:95], v[124:127], v[188:191], v[92:95]\n\t" \
  "v_mfma_f32_16x16x32_bf16 v[88:91], v[124:127], v[184:187], v[88:91]\n\t" \
  "ds_read_b64_tr_b16 v[124:125], v183 offset:16384\n\t" \
  "ds_read_b64_tr_b16 v[126:127], v183 offset:20480\n\t" \
  "s_waitcnt lgkmcnt(6)\n\t" \
  "v_mfma_f32_16x16x32_bf16 v[64:67], v[112:115], v[192:195], v[64:67]\n\t" \
  "v_mfma_f32_16x16x32_bf16 v[68:71], v[112:115], v[196:199], v[68:71]\n\t" \
  "ds_read_b64_tr_b16 v[112:113], v180 offset:32768\n\t" \
  "ds_read_b64_tr_b16 v[114:115], v180 offset:36864\n\t" \
  "s_waitcnt lgkmcnt(6)\n\t" \
  "v_mfma_f32_16x16x32_bf16 v[76:79], v[116:119], v[196:199], v[76:79]\n\t" \
  "v_mfma_f32_16x16x32_bf16 v[72:75], v[116:119], v[192:195], v[72:75]\n\t" \
  "ds_read_b64_tr_b16 v[116:117], v181 offset:32768\n\t" \
  "ds_read_b64_tr_b16 v[118:119], v181 offset:36864\n\t" \
  "s_waitcnt lgkmcnt(6)\n\t" \
  "v_mfma_f32_16x16x32_bf16 v[80:83], v[120:123], v[192:195], v[80:83]\n\t" \
  "v_mfma_f32_16x16x32_bf16 v[84:87], v[120:123], v[196:199], v[84:87]\n\t" \
  "ds_read_b64_tr_b16 v[120:121], v182 offset:32768\n\t" \
  "ds_read_b64_tr_b16 v[122:123], v182 offset:36864\n\t" \
  "s_waitcnt lgkmcnt(6)\n\t" \
  "v_mfma_f32_16x16x32_bf16 v[92:95], v[124:127], v[196:199], v[92:95]\n\t" \
  "v_mfma_f32_16x16x32_bf16 v[88:91], v[124:127], v[192:195], v[88:91]\n\t" \
  "ds_read_b64_tr_b16 v[124:125], v183 offset:32768\n\t" \
  "ds_read_b64_tr_b16 v[126:127], v183 offset:36864\n\t" \
  "s_waitcnt vmcnt(0)\n\t" \
  "s_barrier\n\t" \
  "s_waitcnt lgkmcnt(6)\n\t" \
  "v_mfma_f32_16x16x32_bf16 v[64:67], v[112:115], v[200:203], v[64:67]\n\t" \
  "v_mfma_f32_16x16x32_bf16 v[68:71], v[112:115], v[204:207], v[68:71]\n\t" \
  "ds_read_b64_tr_b16 v[112:113], v180 offset:49152\n\t" \
  "ds_read_b64_tr_b16 v[114:115], v180 offset:53248\n\t" \
  "s_waitcnt lgkmcnt(6)\n\t" \
  "v_mfma_f32_16x16x32_bf16 v[76:79], v[116:119], v[204:207], v[76:79]\n\t" \
  "v_mfma_f32_16x16x32_bf16 v[72:75], v[116:119], v[200:203], v[72:75]\n\t" \
  "ds_read_b64_tr_b16 v[116:117], v181 offset:49152\n\t" \
  "ds_read_b64_tr_b16 v[118:119], v181 offset:53248\n\t" \
  "s_waitcnt lgkmcnt(6)\n\t" \
  "v_mfma_f32_16x16x32_bf16 v[80:83], v[120:123], v[200:203], v[80:83]\n\t" \
  "v_mfma_f32_16x16x32_bf16 v[84:87], v[120:123], v[204:207], v[84:87]\n\t" \
  "ds_read_b64_tr_b16 v[120:121], v182 offset:49152\n\t" \
  "ds_read_b64_tr_b16 v[122:123], v182 offset:53248\n\t" \
  "s_waitcnt lgkmcnt(6)\n\t" \
  "v_mfma_f32_16x16x32_bf16 v[92:95], v[124:127], v[204:207], v[92:95]\n\t" \
  "v_mfma_f32_16x16x32_bf16 v[88:91], v[124:127], v[200:203], v[88:91]\n\t" \
  "ds_read_b64_tr_b16 v[124:125], v183 offset:49152\n\t" \
  "ds_read_b64_tr_b16 v[126:127], v183 offset:53248\n\t" \
  "s_waitcnt lgkmcnt(6)\n\t" \
  "v_mfma_f32_16x16x32_bf16 v[64:67], v[112:115], v[208:211], v[64:67]\n\t" \
  "v_mfma_f32_16x16x32_bf16 v[68:71], v[112:115], v[212:215], v[68:71]\n\t" \
  "ds_read_b64_tr_b16 v[160:161], %[a0] offset:16384\n\t" \
  "ds_read_b64_tr_b16 v[162:163], %[a0] offset:20480\n\t" \
  "s_waitcnt lgkmcnt(6)\n\t" \
  "v_mfma_f32_16x16x32_bf16 v[76:79], v[116:119], v[212:215], v[76:79]\n\t" \
  "v_mfma_f32_16x16x32_bf16 v[72:75], v[116:119], v[208:211], v[72:75]\n\t" \
  "ds_read_b64_tr_b16 v[164:165], %[a1] offset:16384\n\t" \
  "ds_read_b64_tr_b16 v[166:167], %[a1] offset:20480\n\t" \
  "s_waitcnt lgkmcnt(6)\n\t" \
  "v_mfma_f32_16x16x32_bf16 v[80:83], v[120:123], v[208:211], v[80:83]\n\t" \
  "v_mfma_f32_16x16x32_bf16 v[84:87], v[120:123], v[212:215], v[84:87]\n\t" \
  "ds_read_b64_tr_b16 v[168:169], %[a2] offset:16384\n\t" \
  "ds_read_b64_tr_b16 v[170:171], %[a2] offset:20480\n\t" \
  "s_waitcnt lgkmcnt(6)\n\t" \
  "v_mfma_f32_16x16x32_bf16 v[92:95], v[124:127], v[212:215], v[92:95]\n\t" \
  "v_mfma_f32_16x16x32_bf16 v[88:91], v[124:127], v[208:211], v[88:91]\n\t" \
  "ds_read_b64_tr_b16 v[172:173], %[a3] offset:16384\n\t" \
  "ds_read_b64_tr_b16 v[174:175], %[a3] offset:20480\n\t" \
  "v_mfma_f32_16x16x32_bf16 v[32:35], v[96:99], v[128:131], v[32:35]\n\t" \
  "v_mfma_f32_16x16x32_bf16 v[36:39], v[96:99], v[132:135], v[36:39]\n\t" \
  "ds_read_b64_tr_b16 v[96:97], %[a0] offset:32768\n\t" \
  "ds_read_b64_tr_b16 v[98:99], %[a0] offset:36864\n\t" \
  "v_mfma_f32_16x16x32_bf16 v[44:47], v[100:103], v[132:135], v[44:47]\n\t" \
  "v_mfma_f32_16x16x32_bf16 v[40:43], v[100:103], v[128:131], v[40:43]\n\t" \
  "ds_read_b64_tr_b16 v[100:101], %[a1] offset:32768\n\t" \
  "ds_read_b64_tr_b16 v[102:103], %[a1] offset:36864\n\t" \
  "ds_write_b128 %[aw], v[68:71] offset:0\n\t" \
  "ds_write_b128 %[aw], v[76:79] offset:1024\n\t" \
  "ds_write_b128 %[aw], v[84:87] offset:2048\n\t" \
  "ds_write_b128 %[aw], v[92:95] offset:3072\n\t" \
  "v_mfma_f32_16x16x32_bf16 v[48:51], v[104:107], v[128:131], v[48:51]\n\t" \
  "v_mfma_f32_16x16x32_bf16 v[52:55], v[104:107], v[132:135], v[52:55]\n\t" \
  "ds_read_b64_tr_b16 v[104:105], %[a2] offset:32768\n\t" \
  "ds_read_b64_tr_b16 v[106:107], %[a2] offset:36864\n\t" \
  "v_mfma_f32_16x16x32_bf16 v[60:63], v[108:111], v[132:135], v[60:63]\n\t" \
  "v_mfma_f32_16x16x32_bf16 v[56:59], v[108:111], v[128:131], v[56:59]\n\t" \
  "ds_read_b64_tr_b16 v[108:109], %[a3] offset:32768\n\t" \
  "ds_read_b64_tr_b16 v[110:111], %[a3] offset:36864\n\t" \
  "s_waitcnt lgkmcnt(0)\n\t" \
  "s_barrier\n\t" \
  "ds_read_b128 v[112:115], %[ar] offset:0\n\t" \
  "ds_read_b128 v[116:119], %[ar] offset:1024\n\t" \
  "ds_read_b128 v[120:123], %[ar] offset:2048\n\t" \
  "ds_read_b128 v[124:127], %[ar] offset:3072\n\t" \
  "s_waitcnt lgkmcnt(14)\n\t" \
  "v_mfma_f32_16x16x32_bf16 v[32:35], v[160:163], v[136:139], v[32:35]\n\t" \
  "v_mfma_f32_16x16x32_bf16 v[36:39], v[160:163], v[140:143], v[36:39]\n\t" \
  "ds_read_b64_tr_b16 v[160:161], %[a0] offset:49152\n\t" \
  "ds_read_b64_tr_b16 v[162:163], %[a0] offset:53248\n\t" \
  "s_waitcnt lgkmcnt(14)\n\t" \
  "v_mfma_f32_16x16x32_bf16 v[44:47], v[164:167], v[140:143], v[44:47]\n\t" \
  "v_mfma_f32_16x16x32_bf16 v[40:43], v[164:167], v[136:139], v[40:43]\n\t" \
  "ds_read_b64_tr_b16 v[164:165], %[a1] offset:49152\n\t" \
  "ds_read_b64_tr_b16 v[166:167], %[a1] offset:53248\n\t" \
  "s_waitcnt lgkmcnt(0)\n\t" \
  "v_add_f32 v64, v64, v112\n\t" \
  "v_add_f32 v65, v65, v113\n\t" \
  "v_add_f32 v66, v66, v114\n\t" \
  "v_add_f32 v67, v67, v115\n\t" \
  "buffer_store_dwordx4 v[64:67], %[vlin], %[srdD], %[oD] offen offset:0 sc1\n\t" \
  "s_waitcnt lgkmcnt(14)\n\t" \
  "v_mfma_f32_16x16x32_bf16 v[48:51], v[168:171], v[136:139], v[48:51]\n\t" \
  "v_mfma_f32_16x16x32_bf16 v[52:55], v[168:171], v[140:143], v[52:55]\n\t" \
  "ds_read_b64_tr_b16 v[168:169], %[a2] offset:49152\n\t" \
  "ds_read_b64_tr_b16 v[170:171], %[a2] offset:53248\n\t" \
  "v_add_f32 v72, v72, v116\n\t" \
  "v_add_f32 v73, v73, v117\n\t" \
  "v_add_f32 v74, v74, v118\n\t" \
  "v_add_f32 v75, v75, v119\n\t" \
  "buffer_store_dwordx4 v[72:75], %[vlin], %[srdD], %[oD] offen offset:1024 sc1\n\t" \
  "s_waitcnt lgkmcnt(14)\n\t" \
  "v_mfma_f32_16x16x32_bf16 v[60:63], v[172:175], v[140:143], v[60:63]\n\t" \
  "v_mfma_f32_16x16x32_bf16 v[56:59], v[172:175], v[136:139], v[56:59]\n\t" \
  "ds_read_b64_tr_b16 v[172:173], %[a3] offset:49152\n\t" \
  "ds_read_b64_tr_b16 v[174:175], %[a3] offset:53248\n\t" \
  "v_add_f32 v80, v80, v120\n\t" \
  "v_add_f32 v81, v81, v121\n\t" \
  "v_add_f32 v82, v82, v122\n\t" \
  "v_add_f32 v83, v83, v123\n\t" \
  "buffer_store_dwordx4 v[80:83], %[vlin], %[srdD], %[oD] offen offset:2048 sc1\n\t" \
  "s_waitcnt lgkmcnt(14)\n\t" \
  "v_mfma_f32_16x16x32_bf16 v[32:35], v[96:99], v[144:147], v[32:35]\n\t" \
  "v_mfma_f32_16x16x32_bf16 v[36:39], v[96:99], v[148:151], v[36:39]\n\t" \
  "ds_read_b64_tr_b16 v[96:97], v176 offset:0\n\t" \
  "ds_read_b64_tr_b16 v[98:99], v176 offset:4096\n\t" \
  "v_add_f32 v88, v88, v124\n\t" \
  "v_add_f32 v89, v89, v125\n\t" \
  "v_add_f32 v90, v90, v126\n\t" \
  "v_add_f32 v91, v91, v127\n\t" \
  "buffer_store_dwordx4 v[88:91], %[vlin], %[srdD], %[oD] offen offset:3072 sc1\n\t" \
  "s_waitcnt lgkmcnt(14)\n\t" \
  "v_mfma_f32_16x16x32_bf16 v[44:47], v[100:103], v[148:151], v[44:47]\n\t" \
  "v_mfma_f32_16x16x32_bf16 v[40:43], v[100:103], v[144:147], v[40:43]\n\t" \
  "ds_read_b64_tr_b16 v[100:101], v177 offset:0\n\t" \
  "ds_read_b64_tr_b16 v[102:103], v177 offset:4096\n\t" \
  "s_waitcnt lgkmcnt(14)\n\t" \
  "v_mfma_f32_16x16x32_bf16 v[48:51], v[104:107], v[144:147], v[48:51]\n\t" \
  "v_mfma_f32_16x16x32_bf16 v[52:55], v[104:107], v[148:151], v[52:55]\n\t" \
  "ds_read_b64_tr_b16 v[104:105], v178 offset:0\n\t" \
  "ds_read_b64_tr_b16 v[106:107], v178 offset:4096\n\t" \
  "s_waitcnt lgkmcnt(14)\n\t" \
  "v_mfma_f32_16x16x32_bf16 v[60:63], v[108:111], v[148:151], v[60:63]\n\t" \
  "v_mfma_f32_16x16x32_bf16 v[56:59], v[108:111], v[144:147], v[56:59]\n\t" \
  "ds_read_b64_tr_b16 v[108:109], v179 offset:0\n\t" \
  "ds_read_b64_tr_b16 v[110:111], v179 offset:4096\n\t" \
  "s_waitcnt lgkmcnt(14)\n\t" \
  "v_mfma_f32_16x16x32_bf16 v[32:35], v[160:163], v[152:155], v[32:35]\n\t" \
  "v_mfma_f32_16x16x32_bf16 v[36:39], v[160:163], v[156:159], v[36:39]\n\t" \
  "ds_read_b64_tr_b16 v[160:161], v176 offset:16384\n\t" \
  "ds_read_b64_tr_b16 v[162:163], v176 offset:20480\n\t" \
  "s_waitcnt lgkmcnt(14)\n\t" \
  "v_mfma_f32_16x16x32_bf16 v[44:47], v[164:167], v[156:159], v[44:47]\n\t" \
  "v_mfma_f32_16x16x32_bf16 v[40:43], v[164:167], v[152:155], v[40:43]\n\t" \
  "ds_read_b64_tr_b16 v[164:165], v177 offset:16384\n\t" \
  "ds_read_b64_tr_b16 v[166:167], v177 offset:20480\n\t" \
  "s_waitcnt lgkmcnt(14)\n\t" \
  "v_mfma_f32_16x16x32_bf16 v[48:51], v[168:171], v[152:155], v[48:51]\n\t" \
  "v_mfma_f32_16x16x32_bf16 v[52:55], v[168:171], v[156:159], v[52:55]\n\t" \
  "ds_read_b64_tr_b16 v[168:169], v178 offset:16384\n\t" \
  "ds_read_b64_tr_b16 v[170:171], v178 offset:20480\n\t" \
  "s_waitcnt lgkmcnt(14)\n\t" \
  "v_mfma_f32_16x16x32_bf16 v[60:63], v[172:175], v[156:159], v[60:63]\n\t" \
  "v_mfma_f32_16x16x32_bf16 v[56:59], v[172:175], v[152:155], v[56:59]\n\t" \
  "ds_read_b64_tr_b16 v[172:173], v179 offset:16384\n\t" \
  "ds_read_b64_tr_b16 v[174:175], v179 offset:20480\n\t" \
  "s_waitcnt lgkmcnt(14)\n\t" \
  "v_mfma_f32_16x16x32_bf16 v[32:35], v[96:99], v[184:187], v[32:35]\n\t" \
  "v_mfma_f32_16x16x32_bf16 v[36:39], v[96:99], v[188:191], v[36:39]\n\t" \
  "ds_read_b64_tr_b16 v[96:97], v176 offset:32768\n\t" \
  "ds_read_b64_tr_b16 v[98:99], v176 offset:36864\n\t" \
  "s_waitcnt lgkmcnt(14)\n\t" \
  "v_mfma_f32_16x16x32_bf16 v[44:47], v[100:103], v[188:191], v[44:47]\n\t" \
  "v_mfma_f32_16x16x32_bf16 v[40:43], v[100:103], v[184:187], v[40:43]\n\t" \
  "ds_read_b64_tr_b16 v[100:101], v177 offset:32768\n\t" \
  "ds_read_b64_tr_b16 v[102:103], v177 offset:36864\n\t" \
  "s_waitcnt lgkmcnt(14)\n\t" \
  "v_mfma_f32_16x16x32_bf16 v[48:51], v[104:107], v[184:187], v[48:51]\n\t" \
  "v_mfma_f32_16x16x32_bf16 v[52:55], v[104:107], v[188:191], v[52:55]\n\t" \
  "ds_read_b64_tr_b16 v[104:105], v178 offset:32768\n\t" \
  "ds_read_b64_tr_b16 v[106:107], v178 offset:36864\n\t" \
  "s_waitcnt lgkmcnt(14)\n\t" \
  "v_mfma_f32_16x16x32_bf16 v[60:63], v[108:111], v[188:191], v[60:63]\n\t" \
  "v_mfma_f32_16x16x32_bf16 v[56:59], v[108:111], v[184:187], v[56:59]\n\t" \
  "ds_read_b64_tr_b16 v[108:109], v179 offset:32768\n\t" \
  "ds_read_b64_tr_b16 v[110:111], v179 offset:36864\n\t" \
  "s_waitcnt lgkmcnt(14)\n\t" \
  "v_mfma_f32_16x16x32_bf16 v[32:35], v[160:163], v[192:195], v[32:35]\n\t" \
  "v_mfma_f32_16x16x32_bf16 v[36:39], v[160:163], v[196:199], v[36:39]\n\t" \
  "ds_read_b64_tr_b16 v[160:161], v176 offset:49152\n\t" \
  "ds_read_b64_tr_b16 v[162:163], v176 offset:53248\n\t" \
  "s_waitcnt lgkmcnt(14)\n\t" \
  "v_mfma_f32_16x16x32_bf16 v[44:47], v[164:167], v[196:199], v[44:47]\n\t" \
  "v_mfma_f32_16x16x32_bf16 v[40:43], v[164:167], v[192:195], v[40:43]\n\t" \
  "ds_read_b64_tr_b16 v[164:165], v177 offset:49152\n\t" \
  "ds_read_b64_tr_b16 v[166:167], v177 offset:53248\n\t" \
  "s_waitcnt lgkmcnt(14)\n\t" \
  "v_mfma_f32_16x16x32_bf16 v[48:51], v[168:171], v[192:195], v[48:51]\n\t" \
  "v_mfma_f32_16x16x32_bf16 v[52:55], v[168:171], v[196:199], v[52:55]\n\t" \
  "ds_read_b64_tr_b16 v[168:169], v178 offset:49152\n\t" \
  "ds_read_b64_tr_b16 v[170:171], v178 offset:53248\n\t" \
  "v_mov_b32 v112, %[tl]\n\t" \
  "v_mov_b32 v113, %[th]\n\t" \
  "s_waitcnt vmcnt(0)\n\t" \
  "s_mov_b64 exec, 1\n\t" \
  "buffer_store_dwordx2 v[112:113], off, %[srdF], %[oF] sc1\n\t" \
  "s_mov_b64 exec, -1\n\t" \
  "s_waitcnt lgkmcnt(14)\n\t" \
  "v_mfma_f32_16x16x32_bf16 v[60:63], v[172:175], v[196:199], v[60:63]\n\t" \
  "v_mfma_f32_16x16x32_bf16 v[56:59], v[172:175], v[192:195], v[56:59]\n\t" \
  "ds_read_b64_tr_b16 v[172:173], v179 offset:49152\n\t" \
  "ds_read_b64_tr_b16 v[174:175], v179 offset:53248\n\t" \
  "s_waitcnt lgkmcnt(14)\n\t" \
  "v_mfma_f32_16x16x32_bf16 v[32:35], v[96:99], v[200:203], v[32:35]\n\t" \
  "v_mfma_f32_16x16x32_bf16 v[36:39], v[96:99], v[204:207], v[36:39]\n\t" \
  "s_waitcnt lgkmcnt(12)\n\t" \
  "v_mfma_f32_16x16x32_bf16 v[44:47], v[100:103], v[204:207], v[44:47]\n\t" \
  "v_mfma_f32_16x16x32_bf16 v[40:43], v[100:103], v[200:203], v[40:43]\n\t" \
  "s_waitcnt lgkmcnt(10)\n\t" \
  "v_mfma_f32_16x16x32_bf16 v[48:51], v[104:107], v[200:203], v[48:51]\n\t" \
  "v_mfma_f32_16x16x32_bf16 v[52:55], v[104:107], v[204:207], v[52:55]\n\t" \
  "s_waitcnt lgkmcnt(8)\n\t" \
  "v_mfma_f32_16x16x32_bf16 v[60:63], v[108:111], v[204:207], v[60:63]\n\t" \
  "v_mfma_f32_16x16x32_bf16 v[56:59], v[108:111], v[200:203], v[56:59]\n\t" \
  "s_waitcnt lgkmcnt(6)\n\t" \
  "v_mfma_f32_16x16x32_bf16 v[32:35], v[160:163], v[208:211], v[32:35]\n\t" \
  "v_mfma_f32_16x16x32_bf16 v[36:39], v[160:163], v[212:215], v[36:39]\n\t" \
  "s_waitcnt lgkmcnt(4)\n\t" \
  "v_mfma_f32_16x16x32_bf16 v[44:47], v[164:167], v[212:215], v[44:47]\n\t" \
  "v_mfma_f32_16x16x32_bf16 v[40:43], v[164:167], v[208:211], v[40:43]\n\t" \
  "s_waitcnt lgkmcnt(2)\n\t" \
  "v_mfma_f32_16x16x32_bf16 v[48:51], v[168:171], v[208:211], v[48:51]\n\t" \
  "v_mfma_f32_16x16x32_bf16 v[52:55], v[168:171], v[212:215], v[52:55]\n\t" \
  "s_waitcnt lgkmcnt(0)\n\t" \
  "v_mfma_f32_16x16x32_bf16 v[60:63], v[172:175], v[212:215], v[60:63]\n\t" \
  "v_mfma_f32_16x16x32_bf16 v[56:59], v[172:175], v[208:211], v[56:59]\n\t" \
  "s_nop 7\n\t" \
  ""


__device__ __forceinline__ bf16x16 as_b16(const f32x16& p, int half) {
  f32x8 t = half ? __builtin_shufflevector(p, p, 8, 9, 10, 11, 12, 13, 14, 15) : __builtin_shufflevector(p, p, 0, 1, 2, 3, 4, 5, 6, 7);
  return __builtin_bit_cast(bf16x16, t);
}
#define SPIDX 0xDDDDDDDD
#define SMF32(a, b, c) __builtin_amdgcn_smfmac_f32_32x32x32_bf16(a, b, c, (int)SPIDX, 0, 0)
#define SMF16(a, b, c) __builtin_amdgcn_smfmac_f32_16x16x64_bf16(a, b, c, (int)SPIDX, 0, 0)
__device__ __forceinline__ float max16(const f32x16& v) {
  float m = v[0];
#pragma unroll
  for (int r = 1; r < 16; ++r) m = fmaxf(m, v[r]);
  return swap32_max(m);
}

constexpr int LDS_BYTES = 160 * 1024;
constexpr int K5_WOFF = 16384, K5_UOFF = 7168 + 16, K5_SLOT = 16384 + 7168 + 16 + 7168, K5_R = 5;
constexpr int K5_ST = K5_R * K5_SLOT;
constexpr int K5_RS = K5_ST + 2048;
constexpr int K5_UV = K5_RS + 1024;
static_assert(K5_UV + 4 * 112 * 4 <= LDS_BYTES, "LDS");

__global__ __launch_bounds__(512, 2) void block_fwd(const u16* __restrict__ x, const u16* __restrict__ wq, const u16* __restrict__ wk,
                                                    const u16* __restrict__ wv, const u16* __restrict__ wo, const u16* __restrict__ wg,
                                                    const u16* __restrict__ wu, const u16* __restrict__ wd, const u16* __restrict__ c,
                                                    const u16* __restrict__ wmod, u16* __restrict__ outp, char* __restrict__ ws) {
  asm volatile("" :: "s"(x), "s"(wq), "s"(wk), "s"(wv), "s"(c), "s"(wmod), "s"(ws));
  extern __shared__ __attribute__((aligned(16))) char smem_raw[];
  lptr lds = (lptr)smem_raw;
  const unsigned lds0 = (unsigned)(uintptr_t)lds;
  const int w = __builtin_amdgcn_readfirstlane(threadIdx.x >> 6);
  const int bid = blockIdx.x, xcd = bid & 7, loc = bid >> 3;
#define LANE_VARS int lane; asm volatile("v_mbcnt_lo_u32_b32 %0, -1, 0\n\tv_mbcnt_hi_u32_b32 %0, -1, %0" : "=v"(lane)); \
  const int tid = w * 64 + lane, g = lane >> 4, c16 = lane & 15; const unsigned vlin = lane * 16; (void)tid; (void)g; (void)c16; (void)vlin
#define PUBLISH(idx) do { DRAIN_BAR(); if (w == 0) put_flag(sF, (idx) * 8, tag); } while (0)
  gu64* flg = (gu64*)(ws + WS_FLG);
  const rsrc_t rS = make_rsrc(ws, (unsigned)WS_H);
  const u32x4 sF = make_srd(ws + WS_FLG, FL_END * 8);
  const int tg = (xcd & 3) * 4 + (loc & 3);
  const int hp = (xcd >> 2) * 8 + (loc >> 2);
  const int om = (xcd >> 1) * 4 + (loc & 3);
  const int on = (loc >> 2) * 2 + (xcd & 1);
  const int pbid = bid ^ 1;
  u64 tag;
  u32x4 sb4;
  u32x8 rxv;
  u32x8 gbv;
  u32x2 gv[4], cv[4];
#define PIN_RX() asm volatile("" : "+{v[24:31]}"(rxv))

  {
    LANE_VARS;
    const int T16 = bid >> 1, mat = bid & 1;
    const bool storer = (w >> 2) == mat;
    const bool lead = w == 4 * mat;
    const int cgp = ((bid >> 3) & 1) | ((bid & 7) << 1) | (((bid >> 4) & 3) << 4), ks = bid >> 6;
    const int r0 = ks * 256 + (tid >> 2);
    u32x4 wa, wb;
    unsigned ca_, cb_;
    u64 ep;
    asm volatile("buffer_load_dwordx4 %0, %5, %6, 0 offen\n\tbuffer_load_dwordx4 %1, %5, %6, %7 offen\n\t"
                 "buffer_load_ushort %2, %8, %9, 0 offen\n\tbuffer_load_ushort %3, %8, %9, 0 offen offset:256\n\t"
                 "buffer_load_dwordx2 %4, off, %10, 0 sc1"
                 : "=&v"(wa), "=&v"(wb), "=&v"(ca_), "=&v"(cb_), "=&v"(ep)
                 : "v"((r0 * NMOD + cgp * 32 + (tid & 3) * 8) * 2), "s"(make_srd(wmod, DM * NMOD * 2)), "s"(128 * NMOD * 2),
                   "v"(r0 * 2), "s"(make_srd(c, DM * 2)), "s"(sF) : "memory");
    unsigned tc_;
    {
      const char* pc;
      unsigned len;
      asm volatile("s_getpc_b64 %0\n.Lcode_warm_from:\n\ts_mov_b32 %1, .Lcode_warm_to-.Lcode_warm_from" : "=s"(pc), "=s"(len));
      asm volatile("buffer_load_dword %0, %1, %2, 0 offen" : "=&v"(tc_) : "v"((((lane & 1) * 8 + w) * 32 + loc) * 128), "s"(make_srd(pc, len)) : "memory");
    }
#define LOAD_X() do { _Pragma("unroll") for (int r = 0; r < 2; ++r) _Pragma("unroll") for (int i = 0; i < 2; ++i) \
      asm volatile("buffer_load_dwordx4 %0, %1, %2, %3 offen" : "=&v"(v[r][i]) : "v"(vlin), "s"(srdI), "s"(((T16 * 16 + w * 2 + r) * DM + i * 512) * 2) : "memory"); } while (0)
    const u32x4 srdI = make_srd(x, SEQ * DM * 2);
    u32x4 v[2][2];
    asm volatile("s_waitcnt vmcnt(1)" : "+v"(wa), "+v"(wb), "+v"(ca_), "+v"(cb_), "+v"(ep) :: "memory");
    if (!lead) LOAD_X();
    const float s0 = silu_bf(ca_), s1 = silu_bf(cb_);
    LAS float* red = (LAS float*)(lds + 120 * 1024);
    LAS float* slu = (LAS float*)(lds + 123 * 1024);
    if ((tid & 3) == 0) { slu[tid >> 2] = s0; slu[128 + (tid >> 2)] = s1; }
    gemv_rows(wa, wb, s0, s1, red + w * 32, lane);
    tag = ((u64)__builtin_amdgcn_readfirstlane((unsigned)(ep >> 32)) << 32 | (unsigned)__builtin_amdgcn_readfirstlane((unsigned)ep)) + 1;
    LGKM0_BAR();
    if (lead) {
      if (lane < 32) {
        float sum = 0;
#pragma unroll
        for (int i = 0; i < 8; ++i) sum += red[i * 32 + lane];
        bstore1(rS, lane * 4, (unsigned)WS_MODP + (ks * NMOD + cgp * 32) * 4, __float_as_uint(sum));
      }
      asm volatile("s_waitcnt vmcnt(0)" ::: "memory");
      if (lane < 8) __hip_atomic_store(flg + FL_1 + lane * 256 + bid, tag, RLX_AGENT);
      LOAD_X();
    }
    {
      const u32x4 srdW = make_srd(mat ? wv : wk, DM * 32 * 2);
#pragma unroll
      for (int i = 0; i < 8; ++i) dma16(lds0 + (w * 8 + i) * 1024, vlin, srdW, (w * 8 + i) * 1024);
    }
    unsigned tl_;
    {
      const void* p = w == 0 ? (const void*)ws : w == 1 ? (const void*)wg : w == 2 ? (const void*)wu : w == 3 ? (const void*)wd
                      : w == 4 ? (const void*)wmod : w == 5 ? (const void*)wo : w == 6 ? (const void*)outp : (const void*)wq;
      const unsigned n = w == 0 ? (unsigned)WS_END : w < 4 ? DM * FF * 2 : w == 4 ? DM * NMOD * 2 : w == 6 ? SEQ * DM * 2 : DM * DM * 2;
      asm volatile("buffer_load_dword %0, %1, %2, 0 offen" : "=&v"(tl_) : "v"(((lane & 31) * 32 + loc) << 16), "s"(make_srd(p, n)) : "memory");
    }
    unsigned tq_;
    asm volatile("buffer_load_dword %0, %1, %2, 0 offen" : "=&v"(tq_) : "v"((bid * 64 + w * 8 + (lane & 7)) * 128), "s"(make_srd(wq, DM * DM * 2)) : "memory");
    {
      const f32x4 t0 = *(LAS f32x4*)(slu + w * 32 + 4 * g), t1 = *(LAS f32x4*)(slu + w * 32 + 16 + 4 * g);
      sb4 = (u32x4){pack2(t0[0], t0[1]), pack2(t0[2], t0[3]), pack2(t1[0], t1[1]), pack2(t1[2], t1[3])};
    }
    asm volatile("s_waitcnt vmcnt(10)" : "+v"(v[0][0]), "+v"(v[0][1]), "+v"(v[1][0]), "+v"(v[1][1]) :: "memory");
    {
      f32x2 f[2][8], s2[2], q2[2];
#pragma unroll
      for (int r = 0; r < 2; ++r) {
#pragma unroll
        for (int j = 0; j < 8; ++j) f[r][j] = (f32x2){bf_lo(v[r][j >> 2][j & 3]), bf_hi(v[r][j >> 2][j & 3])};
        s2[r] = ((f[r][0] + f[r][1]) + (f[r][2] + f[r][3])) + ((f[r][4] + f[r][5]) + (f[r][6] + f[r][7]));
      }
      const float mm = wave_sum2(s2[0][0] + s2[0][1], s2[1][0] + s2[1][1]) * (1.0f / DM);
#define RL(x, l) __int_as_float(__builtin_amdgcn_readlane(__float_as_int(x), l))
      const float m[2] = {RL(mm, 0), RL(mm, 32)};
#pragma unroll
      for (int r = 0; r < 2; ++r) {
        q2[r] = (f32x2){0, 0};
#pragma unroll
        for (int j = 0; j < 8; ++j) { const f32x2 d = f[r][j] - m[r]; q2[r] += d * d; }
      }
      const float qq = wave_sum2(q2[0][0] + q2[0][1], q2[1][0] + q2[1][1]);
      const float mbv = rbf(mm), iv = __builtin_amdgcn_rcpf(rbf(__builtin_amdgcn_sqrtf(rbf(rbf(qq * (1.0f / DM)) + 1e-5f))));
      const float mb[2] = {RL(mbv, 0), RL(mbv, 32)}, inv[2] = {RL(iv, 0), RL(iv, 32)};
#pragma unroll
      for (int r = 0; r < 2; ++r)
#pragma unroll
        for (int i = 0; i < 2; ++i) {
          u32x4 o;
#pragma unroll
          for (int j = 0; j < 4; ++j) {
            const f32x2 d = f[r][i * 4 + j] - mb[r];
            const unsigned dr = pack2(d[0], d[1]);
            const f32x2 n = (f32x2){bf_lo(dr), bf_hi(dr)} * inv[r];
            o[j] = pack2(n[0], n[1]);
          }
          *(LAS u32x4*)(lds + 65536 + i * 16384 + lane * 256 + ((w * 2 + r + lane) & 15) * 16) = o;
        }
    }
    if (lead) {
      poll4(sF, lane * 8, (FL_1 + xcd * 256) * 8, tag);
      if (bid == 0) put_flag(sF, FL_EPOCH * 8, tag);
    }
    asm volatile("s_waitcnt vmcnt(0) lgkmcnt(0)\n\ts_barrier" :: "v"(tq_), "v"(tl_), "v"(tc_) : "memory");
    bf16x8 wf[4][2];
    f32x2 xf[4][4];
    {
      f32x4 mp[MOD_KS];
#pragma unroll
      for (int k = 0; k < MOD_KS; ++k) mp[k] = bload4f(rS, tid * 16, (unsigned)WS_MODP + k * NMOD * 4);
      __builtin_amdgcn_sched_barrier(0);
      const unsigned aoff = w * 8192 + (8 * g + (c16 >> 2)) * 64 + (lane & 3) * 8;
#pragma unroll
      for (int i = 0; i < 4; ++i) {
        const int ch = (w * 4 + i) * 4 + g;
        const u32x4 xv = *(LAS u32x4*)(lds + 65536 + ch * 256 + ((c16 + ch) & 15) * 16);
#pragma unroll
        for (int a = 0; a < 2; ++a) { wf[i][a] = lds_tr2(lds + aoff + i * 2048 + a * 32, lds + aoff + i * 2048 + a * 32 + 256); EARLY(wf[i][a]); }
#pragma unroll
        for (int j = 0; j < 4; ++j) { xf[i][j] = (f32x2){bf_lo(xv[j]), bf_hi(xv[j])}; EARLY(xf[i][j]); }
      }
      __builtin_amdgcn_sched_barrier(0);
      const f32x4 m = (mp[0] + mp[1]) + (mp[2] + mp[3]);
      const float one = (tid >= 256) ? 1.0f : 0.0f;
      const unsigned r0_ = pack2(m[0], m[1]), r1_ = pack2(m[2], m[3]);
      const unsigned q0_ = pack2(bf_lo(r0_) + one, bf_hi(r0_) + one), q1_ = pack2(bf_lo(r1_) + one, bf_hi(r1_) + one);
      *(LAS f32x4*)(lds + 96 * 1024 + tid * 16) = (f32x4){bf_lo(q0_), bf_hi(q0_), bf_lo(q1_), bf_hi(q1_)};
    }
    const unsigned vWq = (8 * ((lane >> 5) & 1) + ((lane >> 3) & 3)) * (DM * 2) + ((lane & 7) ^ (((lane >> 4) & 1) << 2)) * 16;
    const unsigned sWq = (unsigned)(512 * (w >> 2) + 16 * (w & 3)) * (DM * 2) + hp * 128;
#define WQ_SLOT(i, at) do { dma16(lds0 + w * 2048 + (at), vWq, make_srd(wq, DM * DM * 2), sWq + (i) * 64 * DM * 2); \
      dma16(lds0 + w * 2048 + (at) + 1024, vWq + 4 * DM * 2, make_srd(wq, DM * DM * 2), sWq + (i) * 64 * DM * 2); } while (0)
    WQ_SLOT(4, 131072);
    WQ_SLOT(5, 147456);
    {
      const unsigned vx = ((om * 128 + w * 16 + c16) * DM + on * 64 + 4 * g) * 2;
      asm volatile("buffer_load_dwordx2 v[24:25], %1, %2, 0 offen\n\tbuffer_load_dwordx2 v[26:27], %1, %2, 0 offen offset:32\n\t"
                   "buffer_load_dwordx2 v[28:29], %1, %2, 0 offen offset:64\n\tbuffer_load_dwordx2 v[30:31], %1, %2, 0 offen offset:96"
                   : "=&{v[24:31]}"(rxv) : "v"(vx), "s"(srdI) : "memory");
    }
    LGKM0_BAR();
    f32x4 acc[2] = {{0, 0, 0, 0}, {0, 0, 0, 0}};
    {
      u32x4 hv[4];
#pragma unroll
      for (int i = 0; i < 4; ++i) {
        const int ch = (w * 4 + i) * 4 + g;
        f32x2 sa[4], ca[4];
#pragma unroll
        for (int j = 0; j < 2; ++j) {
          const f32x4 a = *(LAS f32x4*)(lds + 96 * 1024 + ch * 32 + j * 16), cc = *(LAS f32x4*)(lds + 100 * 1024 + ch * 32 + j * 16);
          sa[2 * j] = (f32x2){a[0], a[1]}; sa[2 * j + 1] = (f32x2){a[2], a[3]};
          ca[2 * j] = (f32x2){cc[0], cc[1]}; ca[2 * j + 1] = (f32x2){cc[2], cc[3]};
        }
#pragma unroll
        for (int j = 0; j < 4; ++j) {
          const f32x2 t = xf[i][j] * ca[j];
          const unsigned tr = pack2(t[0], t[1]);
          const f32x2 u = (f32x2){bf_lo(tr), bf_hi(tr)} + sa[j];
          hv[i][j] = pack2(u[0], u[1]);
        }
      }
      if (storer) {
        const rsrc_t rH = make_rsrc(ws + WS_H, SEQ * DM * 2);
#pragma unroll
        for (int i = 0; i < 4; ++i)
          bstore4(rH, (g >> 1) * 1024 + (g & 1) * 512 + c16 * 16, (T16 >> 1) * 65536 + (w * 4 + i) * 2048 + (T16 & 1) * 256, hv[i]);
      }
      if (mat == 0) {
#pragma unroll
        for (int i = 0; i < 4; ++i)
#pragma unroll
          for (int a = 0; a < 2; ++a) acc[a] = MFMA16(wf[i][a], __builtin_bit_cast(bf16x8, hv[i]), acc[a]);
      } else {
#pragma unroll
        for (int i = 0; i < 4; ++i)
#pragma unroll
          for (int a = 0; a < 2; ++a) acc[a] = MFMA16(__builtin_bit_cast(bf16x8, hv[i]), wf[i][a], acc[a]);
      }
    }
    lptr sred = lds + 104 * 1024;
    *(LAS f32x4*)(sred + (w * 2) * 1024 + vlin) = acc[0];
    *(LAS f32x4*)(sred + (w * 2 + 1) * 1024 + vlin) = acc[1];
    asm volatile("s_waitcnt vmcnt(0) lgkmcnt(0)\n\ts_barrier" : "+{v[24:31]}"(rxv) :: "memory");
    if (lead) {
      put_flag(sF, (FL_H + bid) * 8, tag);
      const int q0 = (mat == 0) ? ((g >> 1) * 64 + (g & 1) * 16 + c16) : ((g & 1) * 64 + (g >> 1) * 32 + c16);
      const int q1 = q0 + ((mat == 0) ? 32 : 16);
      f32x4 r0_ = {0, 0, 0, 0}, r1_ = {0, 0, 0, 0};
#pragma unroll
      for (int ww = 0; ww < 8; ++ww) { r0_ += *(LAS f32x4*)(sred + ww * 2048 + q0 * 16); r1_ += *(LAS f32x4*)(sred + ww * 2048 + q1 * 16); }
      const float sc = (mat == 0) ? QSCALE : 1.0f;
      r0_ *= sc; r1_ *= sc;
      const u32x4 p = {pack2(r0_[0], r0_[1]), pack2(r0_[2], r0_[3]), pack2(r1_[0], r1_[1]), pack2(r1_[2], r1_[3])};
      const int T32 = T16 >> 1, odd = T16 & 1;
      if (mat == 0) {
        const unsigned vo = (g >> 1) * 1024 + (g & 1) * 512 + odd * 256 + c16 * 16;
        bstore4(rS, vo, (unsigned)WS_KV + T32 * 4096, p);
        if (T32 == 63) bstore4(rS, vo, (unsigned)WS_KV + 64 * 4096, p);
      } else {
        bstore4(rS, vlin, (unsigned)WS_KV + (T32 + 1) * 4096 + 2048 + odd * 1024, p);
      }
      float red2 = 0, bad = 0;
#pragma unroll
      for (int j = 0; j < 4; ++j) {
        const float a = bf_lo(p[j]), b2 = bf_hi(p[j]);
        red2 = (mat == 0) ? (red2 + a * a + b2 * b2) : fmaxf(red2, fmaxf(fabsf(a), fabsf(b2)));
        bad += a * 0.0f + b2 * 0.0f;
      }
      if (mat == 0) red2 = swap32_sum(swap16_sum(red2));
      if (!(bad == 0.0f) || !(red2 == red2)) red2 = __builtin_inff();
      red2 = wave_max_pos(red2);
      if (lane == 0) bstore1(rS, 0, (unsigned)WS_KST + bid * 4, __float_as_uint(red2));
    }
    WQ_SLOT(0, 0); WQ_SLOT(1, 16384); WQ_SLOT(2, 32768); WQ_SLOT(3, 49152);
  }

  {
    LANE_VARS;
    const int tt = w & 3, kh = w >> 2;
    const int ucg = bid % 112, uks = bid / 112;
    const u32x4 srdK = make_srd(ws + WS_KV + 4096, 64 * 4096);
    const unsigned dk = lds0 + w * 4096;
    unsigned so = (32 * kh + tt) * 4096;
    const bool uvjob = bid < 224;
    const unsigned upitch = uvjob ? FF * 2 : 128;
    const u16* ubase = uvjob ? (ucg < 56 ? wg : wu) : wo;
    const u32x4 srdU = make_srd(ubase, uvjob ? DM * FF * 2 : DM * DM * 2);
    const u32x4 srdP = make_srd(wd, FF * DM * 2);
    unsigned sU = uvjob ? (unsigned)(uks * 512 + w * 32) * (FF * 2) + (ucg < 56 ? ucg : ucg - 56) * 128 : (bid - 224) * 65536 + w * 4096;
    unsigned vU = (lane >> 3) * upitch + ((lane & 7) ^ (g << 1)) * 16, vS = (lane >> 3) * (NMOD * 2) + ((lane & 7) ^ (g << 1)) * 16;
    unsigned aS = lds0 + 131072 + w * 4096 + (4 * g + (c16 >> 2)) * 128 + (((2 * g + (c16 >> 3)) & 3) << 5) + (lane & 3) * 8;
    unsigned ve = (unsigned)WS_MODP + ((c16 & 3) * NMOD + ((c16 & 4) ? 3 : 4) * DM + (uks & 1) * 512 + (w + 8 * (c16 >> 3)) * 32 + 4 * g) * 4;
    unsigned vm = (unsigned)WS_MODP + ((c16 & 3) * NMOD + 2 * DM + on * 64 + (c16 >> 2) * 16 + 4 * g) * 4;
    u32x4 ones;
    {
      const unsigned lo = ((c16 & 7) == 0) ? 0x3f803f80u : 0u, hi = ((c16 & 7) == 4) ? 0x3f803f80u : 0u;
      ones = (u32x4){lo, lo, hi, hi};
    }
    u32x4 kin = {vU, aS, ve, vm};
    asm volatile("" : "+{v[0:3]}"(kin), "+{v[4:7]}"(sb4), "+v"(vS), "+v"(ones), "+s"(sU));
    f32x16 qa, qb;
    u32x4 k0, k1;
    f32x4 kst;
    const u32x4 srdX = make_srd(ws, (unsigned)WS_H);
    {
      const u32x4 srdT = make_srd(ws + WS_H, SEQ * DM * 2);
      const u32x4 srdW = make_srd(wq, DM * DM * 2);
      const int rho = lane >> 3, cp = lane & 7;
      unsigned vW = (8 * ((rho >> 2) & 1) + (rho & 3)) * (DM * 2) + (cp ^ (((rho >> 1) & 1) << 2)) * 16;
      const int q = c16 >> 2, p = lane & 3, g1 = g & 1, h = lane >> 5;
      unsigned a0 = lds0 + kh * 8192 + (4 * h + q) * 128 + ((((q >> 1) & 1) ^ kh) << 6) + g1 * 32 + p * 8;
      unsigned sT = (unsigned)((tg * 4 + tt) * 64 + 32 * kh) * 1024;
      unsigned sW = (unsigned)(512 * kh + 16 * tt + 6 * 64) * (DM * 2) + hp * 128;
      u32x16 ra0, ra1, rt0, rt1, rt2, rt3;
      u64 fl;
      unsigned a1 = a0 ^ 64, b0 = a0 + 131072, b1 = a1 + 131072, vW2 = vW + 4 * DM * 2, vfl = (unsigned)WS_FLG + (FL_K + w * 32 + (lane & 31)) * 8;
      EARLY(a0); EARLY(a1); EARLY(b0); EARLY(b1); EARLY(vW); EARLY(vW2); EARLY(vfl);
      if (w == 2) poll1(sF, c16 * 8, (FL_H + tg * 16) * 8, tag);
      BAR();
      asm volatile(QP_ASM_A
                   : "=&{v[32:47]}"(qa), "=&{v[48:63]}"(qb), "=&{v[64:79]}"(ra0), "=&{v[80:95]}"(ra1), "=&{v[96:111]}"(rt0),
                     "=&{v[112:127]}"(rt1), "=&{v[128:143]}"(rt2), "=&{v[144:159]}"(rt3), [sT] "+s"(sT), [sW] "+s"(sW), [fl] "=&v"(fl)
                   : [a0] "v"(a0), [a1] "v"(a1), [b0] "v"(b0), [b1] "v"(b1), [vW] "v"(vW), [vW2] "v"(vW2), [vlin] "v"(vlin), [vfl] "v"(vfl),
                     [srdT] "s"(srdT), [srdW] "s"(srdW), [srdX] "s"(srdX), [dW] "s"(lds0 + w * 2048),
                     [ld] "s"(w - 4 * (bid & 1)), [tl] "s"((unsigned)tag), [th] "s"((unsigned)(tag >> 32)), [oK] "s"((unsigned)WS_FLG + (FL_K + bid) * 8)
                   : "memory", "scc");
      if (!__builtin_amdgcn_readfirstlane(__all(fl == tag))) poll1(sF, (lane & 31) * 8, (FL_K + w * 32) * 8, tag);
      asm volatile(QP_ASM_B
                   : "+{v[32:47]}"(qa), "+{v[48:63]}"(qb), "+{v[64:79]}"(ra0), "+{v[80:95]}"(ra1), "+{v[96:111]}"(rt0),
                     "+{v[112:127]}"(rt1), "+{v[128:143]}"(rt2), "+{v[144:159]}"(rt3), [so] "+s"(so),
                     [k0] "=&v"(k0), [k1] "=&v"(k1), [kst] "=&v"(kst)
                   : [a0] "v"(a0), [a1] "v"(a1), [vlin] "v"(vlin), [vks] "v"((unsigned)WS_KST + vlin),
                     [srdX] "s"(srdX), [srdK] "s"(srdK), [dk] "s"(dk), [sK0] "s"((unsigned)WS_KV + 32 * kh * 4096)
                   : "memory", "scc");
    }
#pragma unroll
    for (int i = 0; i < 4; ++i) dma16s(dk + 3 * 32768 + i * 1024, vlin, srdK, (32 * kh + tt) * 4096 + 0x4000 + i * 1024);
#pragma unroll
    for (int q = 0; q < 4; ++q) {
      *(LAS f32x4*)(lds + w * 8192 + q * 1024 + lane * 16) = (f32x4){qa[4 * q], qa[4 * q + 1], qa[4 * q + 2], qa[4 * q + 3]};
      *(LAS f32x4*)(lds + w * 8192 + 4096 + q * 1024 + lane * 16) = (f32x4){qb[4 * q], qb[4 * q + 1], qb[4 * q + 2], qb[4 * q + 3]};
    }
    float kmax2 = wave_max_pos(__uint_as_float(max(__float_as_uint(kst[0]), __float_as_uint(kst[2]))));
    float vmax = wave_max_pos(__uint_as_float(max(__float_as_uint(kst[1]), __float_as_uint(kst[3]))));
    asm volatile("" : "+v"(kmax2), "+v"(vmax));
    asm volatile("s_waitcnt vmcnt(4) lgkmcnt(0)\n\ts_barrier" ::: "memory");
#pragma unroll
    for (int q = 0; q < 4; ++q) {
      const f32x4 ta = *(LAS f32x4*)(lds + (w ^ 4) * 8192 + 4096 + q * 1024 + lane * 16);
      const f32x4 tb = *(LAS f32x4*)(lds + (w ^ 4) * 8192 + q * 1024 + lane * 16);
#pragma unroll
      for (int j = 0; j < 4; ++j) { qa[4 * q + j] += ta[j]; qb[4 * q + j] += tb[j]; }
    }
    u32x8 fa, fb;
    f32x2 na2 = {0, 0}, nb2 = {0, 0};
#pragma unroll
    for (int j = 0; j < 8; ++j) {
      const f32x2 xa = {qa[2 * j], qa[2 * j + 1]}, xb = {qb[2 * j], qb[2 * j + 1]};
      fa[j] = pack2(xa[0], xa[1]);
      fb[j] = pack2(xb[0], xb[1]);
      na2 += xa * xa; nb2 += xb * xb;
    }
    const float na = swap32_sum(na2[0] + na2[1]), nb = swap32_sum(nb2[0] + nb2[1]);
    const bf16x8 qa0 = __builtin_bit_cast(bf16x8, __builtin_shufflevector(fa, fa, 0, 1, 2, 3)), qa1 = __builtin_bit_cast(bf16x8, __builtin_shufflevector(fa, fa, 4, 5, 6, 7));
    const bf16x8 qb0 = __builtin_bit_cast(bf16x8, __builtin_shufflevector(fb, fb, 0, 1, 2, 3)), qb1 = __builtin_bit_cast(bf16x8, __builtin_shufflevector(fb, fb, 4, 5, 6, 7));
    f32x16 sa = MFMA32(__builtin_bit_cast(bf16x8, k0), qa0, (f32x16){});
    f32x16 sb = MFMA32(__builtin_bit_cast(bf16x8, k0), qb0, (f32x16){});
    sa = MFMA32(__builtin_bit_cast(bf16x8, k1), qa1, sa);
    sb = MFMA32(__builtin_bit_cast(bf16x8, k1), qb1, sb);
    float ma = max16(sa), mb = max16(sb);
    const float ta_ = 90.0f + ma, tb_ = 90.0f + mb, kb_ = kmax2 * 1.0404f;
    const bool safe = (ta_ > 0.0f) & (tb_ > 0.0f) & (na * kb_ <= ta_ * ta_) & (nb * kb_ <= tb_ * tb_) & (vmax <= 16777216.0f);
    const bool fast = __builtin_amdgcn_readfirstlane(__all(safe)) != 0;
    f32x16 oa, ob;
    f32x4 la, lb;
    unsigned spv = 32768, nx2;
    so = (32 * kh + tt) * 4096 + 3 * 0x4000;
    f32x16 ev;
    {
      const u32x4 srdM = make_srd(wmod, DM * NMOD * 2);
      const unsigned sS = (unsigned)(((bid >> 6) * 256 + w * 32) * NMOD + 2048 + (bid & 63) * 64) * 2;
#pragma unroll
      for (int i = 0; i < 4; ++i) dma16(dk + 131072 + i * 1024, vS, srdM, sS + i * 8 * NMOD * 2);
    }
#define K3_RING_IN [so2] "s"((32 * kh + tt) * 4096 + 2 * 0x4000), [srdK0] "s"((u64)(uintptr_t)(ws + WS_KV + 4096)), \
                     [srdU0] "s"((u64)(uintptr_t)ubase), [lenU] "s"(uvjob ? DM * FF * 2 : DM * DM * 2), [vlin] "v"(vlin), [srdK] "s"(srdK), [dk] "s"(dk), "{v[0:3]}"(kin), "{v[4:7]}"(sb4), [srdU] "s"(srdU), [st8] "s"(8 * upitch), \
                     [sU2] "s"(224 * upitch), [wv] "s"(w), [tl] "s"((unsigned)tag), [th] "s"((unsigned)(tag >> 32)), \
                     [oF] "s"((unsigned)WS_MODP + ((bid >> 6) * NMOD + 2048 + (bid & 63) * 64) * 4), \
                     [o3] "s"((unsigned)WS_FLG + (FL_3 + bid) * 8), [of3] "s"((unsigned)WS_FLG + (FL_3 + w * 32) * 8), \
                     [srdX] "s"(srdX)
    if (fast) {
      unsigned ac = lds0 + 65536 + kh * 16384 + lane * 16, an, sc_, scs = 65536, snx;
      spv = 0;
      asm volatile(K3_ASM_LOOP
                   : "=&{v[112:127]}"(oa), "=&{v[128:143]}"(ob), "=&{v[72:75]}"(la), "=&{v[76:79]}"(lb), "+{v[56:59]}"(k0), "+{v[60:63]}"(k1),
                     [ac] "+v"(ac), [an] "=&v"(an), [c] "=&s"(sc_), [cs] "+s"(scs), [nx] "=&s"(snx), [pv] "+s"(spv),
                     [so] "+s"(so), [sU] "+s"(sU), [nx2] "=&s"(nx2), "=&{v[224:239]}"(ev)
                   : "{v[32:39]}"(fa), "{v[40:47]}"(fb), "{v[48:51]}"(ones), [ma] "v"(ma), [mb] "v"(mb),
                     [kb] "s"(lds0 + kh * 16384), [otch] "s"((bid * 224 + w * 28) * 128), [srdP] "s"(srdP), K3_RING_IN
                   : "memory", "scc", "vcc", "s84", "s85", "s86", "s87", "s88", "s89", "s90", "s91", "s92", "s93", "v52", "v53", "v55", CLOB_64_71, CLOB_80_111, CLOB_144_223, CLOB_240_255);
    } else {
      const bf16x8 onesb = __builtin_bit_cast(bf16x8, ones);
      oa = (f32x16){}; ob = (f32x16){};
      la = (f32x4){0, 0, 0, 0}; lb = (f32x4){0, 0, 0, 0};
#define TILE_SLOW(SC, M, O, L, Q0, Q1) do { \
      const float mn_ = fmaxf(max16(SC), M); \
      const float al_ = __builtin_amdgcn_exp2f(M - mn_); \
      M = mn_; \
      _Pragma("unroll") for (int r = 0; r < 16; ++r) { SC[r] = __builtin_amdgcn_exp2f(SC[r] - mn_); O[r] *= al_; } \
      L[0] *= al_; \
      O = SMF32(vf0, as_b16(SC, 0), O); L = SMF16(onesb, as_b16(SC, 0), L); \
      O = SMF32(vf1, as_b16(SC, 1), O); L = SMF16(onesb, as_b16(SC, 1), L); \
      SC = MFMA32(kf0, Q0, (f32x16){}); SC = MFMA32(kf1, Q1, SC); } while (0)
      unsigned scs = 65536;
      u32x16 fv = {};
      ev = (f32x16){};
      for (unsigned c = 0; c < 8; ++c) {
#pragma unroll 1
        for (int u = 0; u < 4; ++u) {
          if (u == 3) {
            asm volatile("s_waitcnt lgkmcnt(0)\n\t" K3_ASM_RING_SLOW "s_waitcnt lgkmcnt(0)"
                         : [so] "+s"(so), [sU] "+s"(sU), [nx2] "=&s"(nx2), "+{v[224:239]}"(ev), "+{v[240:255]}"(fv)
                         : [c] "s"(c), [pv] "s"(spv), K3_RING_IN : "memory", "scc", "vcc", "s84", "s85", "s86", "s87", "s88", "s89", "v55");
            so = __builtin_amdgcn_readfirstlane(so);
            sU = __builtin_amdgcn_readfirstlane(sU);
          }
          lptr up = lds + scs + kh * 16384 + u * 4096 + lane * 16;
          const bf16x8 kf0 = lds_frag(up), kf1 = lds_frag(up + 1024), vf0 = lds_frag(up + 2048), vf1 = lds_frag(up + 3072);
          TILE_SLOW(sa, ma, oa, la, qa0, qa1);
          TILE_SLOW(sb, mb, ob, lb, qb0, qb1);
        }
        spv = scs;
        scs = (scs + 32768) & 0x1ffff;
      }
      asm volatile("s_waitcnt vmcnt(0)" ::: "memory");
    }
    {
    LANE_VARS;
    {
      lptr xb = lds + 98304 + w * 4096;
#pragma unroll
      for (int q = 0; q < 4; ++q) *(LAS f32x4*)(xb + q * 1024 + vlin) = (f32x4){ob[4 * q], ob[4 * q + 1], ob[4 * q + 2], ob[4 * q + 3]};
      *(LAS f32x2*)(lds + w * 512 + lane * 8) = (f32x2){lb[0], mb};
    }
    LGKM0_BAR();
    {
      lptr xb = lds + 98304 + (w ^ 4) * 4096;
      const f32x2 lm = *(LAS f32x2*)(lds + (w ^ 4) * 512 + lane * 8);
      const float mm = fmaxf(ma, lm[1]);
      float f1 = __builtin_amdgcn_exp2f(ma - mm), f2 = __builtin_amdgcn_exp2f(lm[1] - mm);
      const float inv = __builtin_amdgcn_rcpf(la[0] * f1 + lm[0] * f2);
      f1 *= inv; f2 *= inv;
      u32x4 o[2];
#pragma unroll
      for (int q = 0; q < 4; ++q) {
        const f32x4 t = *(LAS f32x4*)(xb + q * 1024 + vlin);
        const f32x4 v = (f32x4){oa[4 * q], oa[4 * q + 1], oa[4 * q + 2], oa[4 * q + 3]} * f1 + t * f2;
        o[q >> 1][(q & 1) * 2] = pack2(v[0], v[1]);
        o[q >> 1][(q & 1) * 2 + 1] = pack2(v[2], v[3]);
      }
      const rsrc_t srdO = make_rsrc(ws + WS_O, SEQ * DM * 2);
      const unsigned vo = ((g & 1) * 32 * 64 + (lane >> 5) * 16 + c16) * 16;
      const unsigned oo = ((tg * 8 + tt * 2) * 32 + hp * 2 + kh) * 1024;
      bstore4(srdO, vo, oo, o[0]);
      bstore4(srdO, vo, oo + 512, o[1]);
    }
    __builtin_amdgcn_sched_barrier(0);
    {
      u32x4 ef;
      const float o1 = (lane & 4) ? 0.0f : 1.0f;
      float t[8];
#pragma unroll
      for (int j = 0; j < 8; ++j) t[j] = quad_sum(ev[j]);
#pragma unroll
      for (int j = 0; j < 4; ++j) {
        const unsigned r = pack2(t[2 * j], t[2 * j + 1]);
        ef[j] = pack2(bf_lo(r) + o1, bf_hi(r) + o1);
      }
      const int q = c16 >> 2, p = lane & 3, xs = (2 * g + (q >> 1)) & 3;
      lptr ap = lds + 65536 + w * 4096 + (4 * g + q) * 128 + p * 8;
      f32x4 u0[4], u1[4];
#pragma unroll
      for (int a = 0; a < 4; ++a) {
        lptr aq = ap + ((a ^ xs) << 5), ar = aq + 65536;
        u0[a] = MFMA16(lds_tr2(ar, ar + 2048), __builtin_bit_cast(bf16x8, ef), ((f32x4){0, 0, 0, 0}));
        u1[a] = MFMA16(lds_tr2(aq, aq + 2048), __builtin_bit_cast(bf16x8, ef), ((f32x4){0, 0, 0, 0}));
      }
      if ((lane & 3) == 0) {
#pragma unroll
        for (int a = 0; a < 4; ++a)
          *(LAS f32x4*)(lds + 131072 + w * 4096 + (((lane >> 2) & 3) * 64 + a * 16 + 4 * g) * 4) = (lane & 8) ? u1[a] : u0[a];
      }
    }
    mod_spread((f32x4){ev[8], ev[9], ev[10], ev[11]}, 0.0f, gv);
    mod_spread((f32x4){ev[12], ev[13], ev[14], ev[15]}, 1.0f, cv);
#pragma unroll
    for (int a = 0; a < 4; ++a) asm volatile("" : "+v"(gv[a]), "+v"(cv[a]));
    }
    PUBLISH(FL_A + tg * 16 + hp);
    if (uvjob && w == 2) {
      LANE_VARS;
      float su = 0, sv = 0;
#pragma unroll
      for (int i = 0; i < 16; ++i) {
        su += *(LAS float*)(lds + 131072 + (i >> 1) * 4096 + (i & 1) * 512 + lane * 4);
        sv += *(LAS float*)(lds + 131072 + (i >> 1) * 4096 + (i & 1) * 512 + 256 + lane * 4);
      }
      bstore1(rS, lane * 4, (unsigned)WS_UV + (uks * 2 * (2 * FF) + ucg * 64) * 4, __float_as_uint(su));
      bstore1(rS, lane * 4, (unsigned)WS_UV + ((uks * 2 + 1) * (2 * FF) + ucg * 64) * 4, __float_as_uint(sv));
    }
  }

  {
    LANE_VARS;
    const int tm = om, tn = on, kg = w & 1, tq = w >> 1;
    const int T16 = tm * 8 + tq * 2 + kg;
    const u32x4 srdW = make_srd(wo, DM * DM * 2);
    const int rl = lane >> 3, cp = lane & 7;
    const unsigned vW = rl * (DM * 2) + (cp ^ (((rl >> 1) & 3) << 1)) * 16;
    unsigned sW = (unsigned)((w >> 1) * 32 + (w & 1) * 8) * (DM * 2) + tn * 128;
#pragma unroll
    for (int i = 0; i < 6; ++i) {
      dma16(lds0 + w * 2048 + (i < 4 ? i : i + 2) * 16384, vW, srdW, sW + i * 128 * DM * 2);
      dma16(lds0 + w * 2048 + (i < 4 ? i : i + 2) * 16384 + 1024, vW + 16 * DM * 2, srdW, sW + i * 128 * DM * 2);
    }
    sW += 6 * 128 * DM * 2;
    unsigned aa[4], bb[4], vW2 = vW + 16 * DM * 2, vt0 = vlin + kg * 1024 + kg * 32768, vt1 = vlin + kg * 1024 + (1 - kg) * 32768;
    {
      const unsigned ab = kg * 4096 + (4 * g + (c16 >> 2)) * 128 + (((2 * g + (c16 >> 3)) & 3) << 5) + (lane & 3) * 8;
#pragma unroll
      for (int a = 0; a < 4; ++a) { aa[a] = lds0 + (ab ^ (a << 5)); bb[a] = aa[a] + 98304; EARLY(aa[a]); EARLY(bb[a]); }
      EARLY(vW2); EARLY(vt0); EARLY(vt1);
    }
    f32x2 xf[8], gf[8], cf[8];
    PIN_RX();
#pragma unroll
    for (int j = 0; j < 8; ++j) {
      xf[j] = (f32x2){bf_lo(rxv[j]), bf_hi(rxv[j])};
      gf[j] = (f32x2){bf_lo(gv[j >> 1][j & 1]), bf_hi(gv[j >> 1][j & 1])};
      cf[j] = (f32x2){bf_lo(cv[j >> 1][j & 1]), bf_hi(cv[j >> 1][j & 1])};
      EARLY(xf[j]); EARLY(gf[j]); EARLY(cf[j]);
    }
    unsigned ogb = (unsigned)WS_MODP + ((c16 & 3) * NMOD + on * 64 + (c16 >> 2) * 16 + 4 * g) * 4;
    EARLY(ogb);
    if (w == 0) poll1(sF, c16 * 8, (FL_A + tm * 16) * 8, tag);
    BAR();
    f32x16 acc0, acc1;
    {
      const u32x4 srdT = make_srd(ws + WS_O, SEQ * DM * 2);
      unsigned sT = (unsigned)(tm * 8 + tq * 2) * 32 * 1024;
      asm volatile(SG_ASM_LOOP
                   : "=&{v[32:47]}"(acc0), "=&{v[48:63]}"(acc1), [sT] "+s"(sT), [sW] "+s"(sW)
                   : [a0] "v"(aa[0]), [a1] "v"(aa[1]), [a2] "v"(aa[2]), [a3] "v"(aa[3]), [b0] "v"(bb[0]), [b1] "v"(bb[1]), [b2] "v"(bb[2]), [b3] "v"(bb[3]),
                     [vW] "v"(vW), [vW2] "v"(vW2), [vt0] "v"(vt0), [vt1] "v"(vt1), [srdT] "s"(srdT), [srdW] "s"(srdW), [dW] "s"(lds0 + w * 2048),
                     [srdX] "s"(make_srd(ws, (unsigned)WS_H)), [ld] "s"(w - 2), [tl] "s"((unsigned)tag), [th] "s"((unsigned)(tag >> 32)),
                     [oK] "s"((unsigned)WS_FLG + (FL_U + bid) * 8)
                   : "memory", "scc", CLOB_64_95, CLOB_96_159);
    }
    const f32x4 pgb = bload4f(rS, ogb, 5 * DM * 4);
    __builtin_amdgcn_sched_barrier(0);
    f32x4 s[4];
    {
      lptr xb = lds + 64 * 1024;
#pragma unroll
      for (int a = 0; a < 4; ++a) {
        const f32x16& A = (a < 2) ? acc0 : acc1;
        const int o = (a & 1) * 8;
        *(LAS f32x4*)(xb + w * 4096 + a * 1024 + vlin) = (f32x4){A[o + 4], A[o + 5], A[o + 6], A[o + 7]};
        s[a] = (f32x4){A[o], A[o + 1], A[o + 2], A[o + 3]};
      }
      LGKM0_BAR();
#pragma unroll
      for (int a = 0; a < 4; ++a) s[a] += *(LAS f32x4*)(xb + (w ^ 1) * 4096 + a * 1024 + vlin);
    }
    f32x2 f[8];
    u32x8 y;
#pragma unroll
    for (int j = 0; j < 8; ++j) {
      const f32x2 t = xf[j] + gf[j] * (f32x2){s[j >> 1][(j & 1) * 2], s[j >> 1][(j & 1) * 2 + 1]};
      rxv[j] = pack2(t[0], t[1]);
      f[j] = (f32x2){bf_lo(rxv[j]), bf_hi(rxv[j])};
      const f32x2 u = f[j] * cf[j];
      y[j] = pack2(u[0], u[1]);
    }
    PIN_RX();
    const unsigned fo = (T16 * 32 + tn * 2) * 1024;
    const rsrc_t rY = make_rsrc(ws + WS_Y, SEQ * DM * 2);
    bstore4(rY, vlin, fo, (u32x4){y[0], y[1], y[2], y[3]});
    bstore4(rY, vlin, fo + 1024, (u32x4){y[4], y[5], y[6], y[7]});
    asm volatile("s_nop 1" ::: "memory");
    __builtin_amdgcn_sched_barrier(0);
    const f32x2 s2 = ((f[0] + f[1]) + (f[2] + f[3])) + ((f[4] + f[5]) + (f[6] + f[7]));
    const float mu = swap32_sum(swap16_sum(s2[0] + s2[1])) * (1.0f / 64.0f);
    f32x2 qa = {0, 0}, qb = {0, 0};
#pragma unroll
    for (int j = 0; j < 8; j += 2) { const f32x2 d = f[j] - mu, e = f[j + 1] - mu; qa += d * d; qb += e * e; }
    qa += qb;
    const float q2 = swap32_sum(swap16_sum(qa[0] + qa[1]));
    if (g == 0) bstore2(rS, c16 * 8, (unsigned)WS_ST2 + (tn * SEQ + T16 * 16) * 8, (u32x2){__float_as_uint(mu), __float_as_uint(q2)});
    __builtin_amdgcn_sched_barrier(0);
    {
      u32x2 t[4];
      mod_spread(pgb, 0.0f, t);
      gbv = (u32x8){t[0][0], t[0][1], t[1][0], t[1][1], t[2][0], t[2][1], t[3][0], t[3][1]};
      asm volatile("" : "+{v[16:23]}"(gbv));
    }
    PUBLISH(FL_O + tm * 16 + tn);
  }

  {
    LANE_VARS;
    const int tm = (xcd & 1) * 4 + (loc & 3);
    const int tn = (xcd >> 1) * 8 + (loc >> 2);
    const int mq = w >> 1, nh = w & 1;
    const u32x4 srdY = make_srd(ws + WS_Y, SEQ * DM * 2);
    const u32x4 srdG = make_srd(wg, DM * FF * 2);
    const u32x4 srdU = make_srd(wu, DM * FF * 2);
    const int wi = (w < 7) ? w : 6;
    unsigned vW;
    {
      const int cidx = wi * 64 + lane, rho = cidx / 14, cc = cidx - rho * 14;
      vW = rho * (FF * 2) + cc * 16;
    }
    const unsigned sT0 = (unsigned)(tm * 16 + 2 * w) * 32768, sW0 = tn * 224;
    const unsigned dT = lds0 + w * 2048, dW = lds0 + K5_WOFF + wi * 1024;
#pragma unroll
    for (int i = 0; i < 4; ++i) {
      dma16(dW + i * K5_SLOT, vW, srdG, sW0 + i * (32 * FF * 2));
      dma16(dW + i * K5_SLOT + K5_UOFF, vW, srdU, sW0 + i * (32 * FF * 2));
    }
    unsigned o1 = (unsigned)(WS_ST2 - WS_UV) + ((tid & 3) * 4 * SEQ + tm * 256 + (tid >> 2) * 2) * 8, o2;
    {
      const int i = (tid < 112) ? tid : 0, r = i / 28;
      o2 = ((r >> 1) * 2 * FF + (r & 1) * FF + tn * 112 + (i - r * 28) * 4) * 4;
    }
    EARLY(o1); EARLY(o2);
    if (w == 0) poll1(sF, (lane & 31) * 8, (FL_O + tm * 32) * 8, tag);
    if (w == 1) poll4(sF, lane * 8, FL_U * 8, tag);
    BAR();
#define K5_Y(s) do { dma16s(dT + (s) * K5_SLOT, vlin, srdY, sT0 + (s) * 1024); dma16s(dT + (s) * K5_SLOT + 1024, vlin, srdY, sT0 + 32768 + (s) * 1024); } while (0)
    f32x4 sv[4], uv0, uv1;
    {
      const u32x4 srdS = make_srd(ws + WS_UV, (unsigned)(WS_KV - WS_UV));
#pragma unroll
      for (int j = 0; j < 4; ++j) asm volatile("buffer_load_dwordx4 %0, %1, %2, %3 offen sc1" : "=&v"(sv[j]) : "v"(o1), "s"(srdS), "s"(j * SEQ * 8) : "memory");
      asm volatile("buffer_load_dwordx4 %0, %2, %3, %4 offen sc1\n\tbuffer_load_dwordx4 %1, %2, %3, %5 offen sc1"
                   : "=&v"(uv0), "=&v"(uv1) : "v"(o2), "s"(srdS), "s"(w < 2 ? 0 : 0x40000000), "s"(w < 2 ? 4 * FF * 4 : 0x40000000) : "memory");
    }
    K5_Y(0);
    unsigned aoff = lds0 + K5_WOFF + (lane & 1) * K5_UOFF + (4 * g + (c16 >> 2)) * 224 + (64 * nh + 4 * ((lane >> 1) & 1)) * 2;
    unsigned boff = lds0 + mq * 4096 + vlin, vlin2 = vlin + 32768, aS = ((tid & 1) ? K5_RS : K5_ST) + (tid >> 2) * 8;
    unsigned aR = lds0 + K5_RS + (mq * 64 + (lane & 31)) * 4, aV = lds0 + K5_UV + 896 + (64 * nh + 4 * (g >> 1)) * 4;
    unsigned vo = ((g & 1) * 112 * 64 + (g >> 1) * 16 + c16) * 16;
    u32x2 aTU = {lds0 + K5_ST + (mq * 64 + c16) * 4, lds0 + K5_UV + ((g & 1) * 112 + 64 * nh + 4 * (g >> 1)) * 4};
    u32x8 gin = {aoff, boff, vlin, vlin2, vW, aR, aV, vo};
    EARLY(aS);
    asm volatile("" : "+{v[240:241]}"(aTU), "+{v[0:7]}"(gin));
    asm volatile("s_waitcnt vmcnt(2)" : "+v"(sv[0]), "+v"(sv[1]), "+v"(sv[2]), "+v"(sv[3]), "+v"(uv0), "+v"(uv1) :: "memory");
    K5_Y(1);
    {
      const f32x4 sum = (sv[0] + sv[1]) + (sv[2] + sv[3]);
      const f32x2 mean = (f32x2){quad_sum(sum[0]), quad_sum(sum[2])} * (1.0f / 16.0f);
      f32x2 d2 = {0, 0};
#pragma unroll
      for (int j = 0; j < 4; ++j) { const f32x2 d = (f32x2){sv[j][0], sv[j][2]} - mean; d2 += d * d; }
      const f32x2 tot = (f32x2){sum[1], sum[3]} + d2 * 64.0f;
      const float v0 = quad_sum(tot[0]) * (1.0f / DM) + 1e-5f, v1 = quad_sum(tot[1]) * (1.0f / DM) + 1e-5f;
      const f32x2 o = (tid & 1) ? (f32x2){__builtin_amdgcn_rsqf(v0), __builtin_amdgcn_rsqf(v1)} : -mean;
      *(LAS f32x2*)(lds + aS) = o;
      if (tid < 112) *(LAS f32x4*)(lds + K5_UV + tid * 16) = uv0 + uv1;
    }
    asm volatile("s_waitcnt vmcnt(2) lgkmcnt(0)\n\ts_barrier" ::: "memory");
    {
      unsigned rd = K5_SLOT, wr = 4 * K5_SLOT, sT = sT0 + 4 * 1024, sW = sW0 + 4 * (32 * FF * 2), cnt;
      const unsigned oP = (tm * 16 + mq * 4) * 112 * 1024 + (7 * tn + 4 * nh) * 512, oL = (tm * 16 + mq * 4) * 112 * 1024 + (7 * tn + 3) * 512 + 8 * nh;
      asm volatile(K5_ASM_LOOP
                   : "+{v[24:31]}"(rxv), "+{v[16:23]}"(gbv), "+{v[240:241]}"(aTU), [rd] "+s"(rd), [wr] "+s"(wr), [sT] "+s"(sT), [sW] "+s"(sW), [cnt] "=&s"(cnt)
                   : "{v[0:7]}"(gin), [d6] "s"(nh ? -16 : 96), [e6] "s"(nh ? -32 : 192),
                     [srdY] "s"(srdY), [srdG] "s"(srdG), [srdU] "s"(srdU), [dT] "s"(dT), [dW] "s"(dW),
                     [srdA] "s"(make_srd(ws + WS_ACT, SEQ * FF * 2)), [oP0] "s"(oP), [oP1] "s"(oP + 2 * 112 * 1024), [oL0] "s"(oL), [oL1] "s"(oL + 2 * 112 * 1024)
                   : "memory", "scc", CLOB_G);
    }
    PUBLISH(FL_G + tm * 32 + tn);
  }

  {
    LANE_VARS;
    const int kh = on & 1;
    const int tm = om, tw = on >> 1, tn = on;
    const int kg = w & 1, tq = w >> 1;
    const int T16 = tm * 8 + tq * 2 + kg, tok = T16 * 16 + c16;
    const u32x4 srdW = make_srd(wd, (unsigned)FF * DM * 2);
    const unsigned vW = g * (DM * 2) + (c16 ^ (g << 1)) * 16, vW2 = (4 + g) * (DM * 2) + (c16 ^ ((4 + g) << 1)) * 16;
    unsigned sW = (unsigned)(kh * 1792 + (w >> 2) * 32 + (w & 1) * 16 + ((w >> 1) & 1) * 8) * (DM * 2) + tw * 256;
#pragma unroll
    for (int i = 0; i < 4; ++i) {
      dma16(lds0 + w * 2048 + i * 16384, vW, srdW, sW + i * 64 * DM * 2);
      dma16(lds0 + w * 2048 + i * 16384 + 1024, vW2, srdW, sW + i * 64 * DM * 2);
    }
    sW += 4 * 64 * DM * 2;
    unsigned aa[8], vt0 = vlin + kg * 1024 + kg * 112 * 1024, vt1 = vlin + kg * 1024 + (1 - kg) * 112 * 1024;
    unsigned aw = lds0 + 131072 + w * 4096 + vlin, ar = lds0 + 131072 + (w ^ 1) * 4096 + vlin;
    {
      const unsigned ab = kg * 8192 + (4 * g + (c16 >> 2)) * 256 + (((4 * (g & 1) + (c16 >> 2)) ^ (4 * kh)) << 5) + (lane & 3) * 8;
#pragma unroll
      for (int a = 0; a < 8; ++a) { aa[a] = lds0 + (ab ^ (a << 5)); EARLY(aa[a]); }
      EARLY(vt0); EARLY(vt1); EARLY(aw); EARLY(ar);
    }
    if (w == 0) poll1(sF, c16 * 8, (FL_G + (tm >> 1) * 32 + kh * 16) * 8, tag);
    BAR();
    f32x16 acc[2];
    {
      const u32x4 srdT = make_srd(ws + WS_ACT, (unsigned)SEQ * FF * 2);
      unsigned sT = (unsigned)((tm * 8 + tq * 2) * 112 + kh * 56) * 1024, cnt, dW = lds0 + w * 2048;
      asm volatile(DN_ASM_LOOP
                   : "=&{v[32:47]}"(acc[0]), "=&{v[48:63]}"(acc[1]), "+{v[24:31]}"(rxv), "+{v[16:23]}"(gbv),
                     [sT] "+s"(sT), [sW] "+s"(sW), [cnt] "=&s"(cnt), [dW] "+s"(dW),
                     [a0] "+v"(aa[0]), [a1] "+v"(aa[1]), [a2] "+v"(aa[2]), [a3] "+v"(aa[3]), [a4] "+v"(aa[4]), [a5] "+v"(aa[5]), [a6] "+v"(aa[6]), [a7] "+v"(aa[7])
                   : [vW] "v"(vW), [vW2] "v"(vW2), [vt0] "v"(vt0), [vt1] "v"(vt1), [vlin] "v"(vlin), [srdT] "s"(srdT), [srdW] "s"(srdW),
                     [aw] "v"(aw), [ar] "v"(ar),
                     [srdD] "s"(make_srd(ws + WS_XD, 256 * 32768)), [oD] "s"((bid * 8 + w) * 4096),
                     [srdF] "s"(sF), [oF] "s"((FL_D + bid * 16 + w) * 8), [tl] "s"((unsigned)tag), [th] "s"((unsigned)(tag >> 32))
                   : "memory", "scc", CLOB_64_95, CLOB_96_159, CLOB_160_215);
    }
    f32x4 s[4];
    {
      const rsrc_t rD = make_rsrc(ws + WS_XD, 256 * 32768);
      poll1(sF, 0, (FL_D + pbid * 16 + w) * 8, tag);
      f32x4 p[4];
#pragma unroll
      for (int a = 0; a < 4; ++a) p[a] = bload4f(rD, vlin, (pbid * 8 + w) * 4096 + a * 1024);
      __builtin_amdgcn_sched_barrier(0);
      BAR();
#pragma unroll
      for (int a = 0; a < 4; ++a) {
        const f32x16& A = acc[a >> 1];
        const int o = (a & 1) * 8;
        *(LAS f32x4*)(lds + 131072 + w * 4096 + a * 1024 + vlin) = (f32x4){A[o + 4], A[o + 5], A[o + 6], A[o + 7]};
        s[a] = (f32x4){A[o], A[o + 1], A[o + 2], A[o + 3]};
      }
      LGKM0_BAR();
#pragma unroll
      for (int a = 0; a < 4; ++a) s[a] += *(LAS f32x4*)(lds + 131072 + (w ^ 1) * 4096 + a * 1024 + vlin) + p[a];
    }
    PIN_RX();
    asm volatile("" : "+{v[16:23]}"(gbv));
    const u32x2 rx[4] = {{rxv[0], rxv[1]}, {rxv[2], rxv[3]}, {rxv[4], rxv[5]}, {rxv[6], rxv[7]}};
    const u32x2 gv[4] = {{gbv[0], gbv[1]}, {gbv[2], gbv[3]}, {gbv[4], gbv[5]}, {gbv[6], gbv[7]}};
    u32x2 o[4];
#pragma unroll
    for (int a = 0; a < 4; ++a) {
      o[a][0] = pack2(bf_lo(rx[a][0]) + bf_lo(gv[a][0]) * s[a][0], bf_hi(rx[a][0]) + bf_hi(gv[a][0]) * s[a][1]);
      o[a][1] = pack2(bf_lo(rx[a][1]) + bf_lo(gv[a][1]) * s[a][2], bf_hi(rx[a][1]) + bf_hi(gv[a][1]) * s[a][3]);
    }
#pragma unroll
    for (int a = 0; a < 4; a += 2) {
      auto r0 = __builtin_amdgcn_permlane16_swap(o[a][0], o[a + 1][0], false, false);
      auto r1 = __builtin_amdgcn_permlane16_swap(o[a][1], o[a + 1][1], false, false);
      __builtin_amdgcn_raw_buffer_store_b128((u32x4){r0[0], r1[0], r0[1], r1[1]}, make_rsrc(outp, SEQ * DM * 2), (tok * DM + tn * 64 + (a + (g & 1)) * 16 + (g >> 1) * 8) * 2, 0, OUT_AUX);
    }
  }
  asm volatile(".Lcode_warm_to:");
}

extern "C" void kernel_launch(void* const* d_in, const int* in_sizes, int n_in, void* d_out, int out_size,
                              void* d_ws, size_t ws_size, hipStream_t stream) {
  (void)hipFuncSetAttribute((const void*)block_fwd, hipFuncAttributeMaxDynamicSharedMemorySize, LDS_BYTES);
  block_fwd<<<256, 512, LDS_BYTES, stream>>>((const u16*)d_in[0], (const u16*)d_in[1], (const u16*)d_in[2], (const u16*)d_in[3],
                                             (const u16*)d_in[4], (const u16*)d_in[6], (const u16*)d_in[7], (const u16*)d_in[8],
                                             (const u16*)d_in[9], (const u16*)d_in[10], (u16*)d_out, (char*)d_ws);
}
```
